```python
import torch

def _softmax(x, axis=-1):
    return torch.softmax(x, dim=axis)
def _gelu(x):
    return torch.nn.functional.gelu(x, approximate="tanh")
def _silu(x):
    return torch.nn.functional.silu(x)
def _sigmoid(x):
    return torch.sigmoid(x)
def _layernorm(x, axis=-1, eps=1e-5):
    m = x.mean(dim=axis, keepdim=True)
    v = x.var(dim=axis, keepdim=True, unbiased=False)
    return (x - m) / torch.sqrt(v + eps)
def _scatter_add(tgt, idx, src, axis=0):
    return tgt.clone().index_add_(axis, idx, src)
def _segment_sum(src, seg, n):
    out = torch.zeros((n,) + src.shape[1:], dtype=src.dtype)
    return out.index_add_(0, seg, src)
def _pad(x, pw):
    flat = tuple(v for pair in reversed(pw) for v in pair)
    return torch.nn.functional.pad(x, flat)

kernel_name = 'gated_mix__linear_kernel_hedgehog__mixer_token_mlp__prompt'

def setup_inputs():

    _w = (lambda a: (lambda t: t.to(torch.bfloat16) if t.is_floating_point() else t)(torch.as_tensor(a)))
    def _A_setup():
        B, S, D, H = 1, 2048, 512, 32
        Dh = D // H
        return {
            "x": _w((lambda *s: torch.randn(*s, dtype=torch.bfloat16) * 0.05)(B, S, D)),
            "wqkv": _w((lambda *s: torch.randn(*s, dtype=torch.bfloat16) * 0.8)(D, 3 * D)),
            "w_phi": _w((lambda *s: torch.randn(*s, dtype=torch.bfloat16) * 0.8)(H, Dh, Dh) * (Dh ** -0.5)),
            "wo": _w((lambda *s: torch.randn(*s, dtype=torch.bfloat16) * 0.05)(D, D)),
        }
    _A_in = _A_setup()

    def _B_setup():
        B, S, D, F = 1, 2048, 512, 1792
        return {
            "x": _w((lambda *s: torch.randn(*s, dtype=torch.bfloat16) * 0.05)(B, S, D)),
            "w1": _w((lambda *s: torch.randn(*s, dtype=torch.bfloat16) * 0.4)(S, F) * (S ** -0.5)),
            "b1": _w((lambda *s: torch.randn(*s, dtype=torch.bfloat16) * 3.2)(F) * 0.02),
            "w2": _w((lambda *s: torch.randn(*s, dtype=torch.bfloat16) * 0.1)(F, S) * (F ** -0.5)),
            "b2": _w((lambda *s: torch.randn(*s, dtype=torch.bfloat16) * 0.4)(S) * 0.02),
        }
    _B_in = _B_setup()

    return {
        "A_x": _A_in["x"],
        "A_wqkv": _A_in["wqkv"],
        "A_w_phi": _A_in["w_phi"],
        "A_wo": _A_in["wo"],
        "B_x": _B_in["x"],
        "B_w1": _B_in["w1"],
        "B_b1": _B_in["b1"],
        "B_w2": _B_in["w2"],
        "B_b2": _B_in["b2"],
        "gate_w": _w((lambda *s: torch.randn(*s, dtype=torch.bfloat16) * 0.05)(512) * 16.0),
    }

def reference(A_x, A_wqkv, A_w_phi, A_wo, B_x, B_w1, B_b1, B_w2, B_b2, gate_w):
    def _A_ref(x, wqkv, w_phi, wo):
        B, S, D = x.shape
        H = 32; Dh = D // H
        qkv = (lambda x, *d: x.permute(*d))((x @ wqkv).reshape(B, S, 3, H, Dh), 2, 0, 3, 1, 4)
        q, k, v = qkv[0], qkv[1], qkv[2]
        pq = torch.einsum("bhsd,hdf->bhsf", q, w_phi)
        pk = torch.einsum("bhsd,hdf->bhsf", k, w_phi)
        phi_q = torch.concatenate([_softmax(pq, axis=-1), _softmax(-pq, axis=-1)], axis=-1)
        phi_k = torch.concatenate([_softmax(pk, axis=-1), _softmax(-pk, axis=-1)], axis=-1)
        kv = torch.einsum("bhsf,bhsd->bhfd", phi_k, v)
        z  = torch.einsum("bhsf->bhf", phi_k)
        num = torch.einsum("bhqf,bhfd->bhqd", phi_q, kv)
        den = torch.einsum("bhqf,bhf->bhq", phi_q, z)[..., None] + 1e-6
        o = num / den
        return (lambda x, *d: x.permute(*d))(o, 0, 2, 1, 3).reshape(B, S, D) @ wo

    def _B_ref(x, w1, b1, w2, b2):
        B, S, D = x.shape
        h = _layernorm(x, axis=-1)
        u = torch.einsum("bsd,sf->bdf", h, w1) + b1
        u = _gelu(u)
        y = torch.einsum("bdf,fs->bds", u, w2) + b2
        return (lambda x, *d: x.permute(*d))(y, 0, 2, 1)

    x = A_x
    g = _sigmoid((x * gate_w).sum(-1, keepdims=True))
    a = _A_ref(x, A_wqkv, A_w_phi, A_wo)
    b = _B_ref(x, B_w1, B_b1, B_w2, B_b2)
    return g * a + (1 - g) * b
```

```cpp
#include <hip/hip_runtime.h>
#include <hip/hip_bf16.h>

typedef unsigned short u16;
typedef unsigned int u32;
typedef __attribute__((ext_vector_type(8))) __bf16 bf16x8;
typedef __attribute__((ext_vector_type(2))) __bf16 bf16x2;
typedef __attribute__((ext_vector_type(4))) short s16x4;
typedef __attribute__((ext_vector_type(4))) float f32x4;
typedef __attribute__((ext_vector_type(2))) float f32x2;
typedef __attribute__((ext_vector_type(4))) unsigned int u32x4;
typedef __attribute__((ext_vector_type(2))) unsigned int u32x2;
typedef __amdgpu_buffer_rsrc_t rsrc_t;

#define DEVI __device__ __forceinline__
#define SCHED_FENCE() __builtin_amdgcn_sched_barrier(0)
#define RAW_BARRIER() do { asm volatile("s_waitcnt lgkmcnt(0)" ::: "memory"); __builtin_amdgcn_s_barrier(); asm volatile("" ::: "memory"); } while (0)

constexpr int S_ = 2048, D_ = 512, F_ = 1792;
constexpr size_t WS_H = 0;
constexpr size_t WS_PHIQ = WS_H + 2097152;
constexpr size_t WS_KVP = WS_PHIQ + 4194304;
constexpr size_t WS_ZP = WS_KVP + 2097152;
constexpr size_t WS_G = WS_ZP + 131072;
constexpr size_t WS_U = WS_G + 8192;
constexpr size_t WS_O = WS_U + 1835008;

DEVI rsrc_t mkrs(const void* p, int bytes) { return __builtin_amdgcn_make_buffer_rsrc((void*)p, (short)0, bytes, 0x00020000); }
DEVI u32x4 ld16(rsrc_t r, int v, int s) { return __builtin_amdgcn_raw_buffer_load_b128(r, v, s, 0); }
DEVI u32x2 ld8(rsrc_t r, int v, int s) { return __builtin_amdgcn_raw_buffer_load_b64(r, v, s, 0); }
DEVI u32 pk2(float lo, float hi) { f32x2 v = {lo, hi}; return __builtin_bit_cast(u32, __builtin_convertvector(v, bf16x2)); }
DEVI float blo(u32 v) { return __builtin_bit_cast(float, v << 16); }
DEVI float bhi(u32 v) { return __builtin_bit_cast(float, v & 0xffff0000u); }
DEVI float rbf(float x) { return blo(pk2(x, x)); }
DEVI f32x4 mfma(u32x4 a, u32x4 b, f32x4 c) {
  return __builtin_amdgcn_mfma_f32_16x16x32_bf16(__builtin_bit_cast(bf16x8, a), __builtin_bit_cast(bf16x8, b), c, 0, 0, 0);
}
DEVI void wperm(const u32x2* w, u32x4* f) {
#pragma unroll
  for (int t = 0; t < 4; ++t) {
    f[0][t] = __builtin_amdgcn_perm(w[2 * t + 1].x, w[2 * t].x, 0x05040100u);
    f[1][t] = __builtin_amdgcn_perm(w[2 * t + 1].x, w[2 * t].x, 0x07060302u);
    f[2][t] = __builtin_amdgcn_perm(w[2 * t + 1].y, w[2 * t].y, 0x05040100u);
    f[3][t] = __builtin_amdgcn_perm(w[2 * t + 1].y, w[2 * t].y, 0x07060302u);
  }
}
template <int CTRL> DEVI float dppf(float x) {
  return __builtin_bit_cast(float, __builtin_amdgcn_update_dpp(0, __builtin_bit_cast(int, x), CTRL, 0xf, 0xf, true));
}
DEVI float wave_sum(float x) {
  x += dppf<0xB1>(x); x += dppf<0x4E>(x); x += dppf<0x141>(x); x += dppf<0x140>(x);
  u32 b = __builtin_bit_cast(u32, x);
  auto r = __builtin_amdgcn_permlane16_swap(b, b, false, false);
  x = __builtin_bit_cast(float, (u32)r[0]) + __builtin_bit_cast(float, (u32)r[1]);
  b = __builtin_bit_cast(u32, x);
  auto q = __builtin_amdgcn_permlane32_swap(b, b, false, false);
  return __builtin_bit_cast(float, (u32)q[0]) + __builtin_bit_cast(float, (u32)q[1]);
}
DEVI u32x2 trread(const char* p) {
  s16x4 v = __builtin_amdgcn_ds_read_tr16_b64_v4i16((__attribute__((address_space(3))) s16x4*)(p));
  return __builtin_bit_cast(u32x2, v);
}

constexpr int HS = 1152;
constexpr int L1_X = 0;
constexpr int L1_T = 65536;
constexpr int L1_PH = L1_T + 24 * HS;
constexpr int L1_WF = L1_PH + 16 * HS;
constexpr int L1_SIZE = L1_WF + 8192;
constexpr int PD1 = 6;

__global__ __launch_bounds__(512, 1) void k1(const u16* __restrict__ x, const u16* __restrict__ wqkv,
                                              const u16* __restrict__ wphi, const u16* __restrict__ gw,
                                              char* __restrict__ ws) {
  __shared__ __attribute__((aligned(16))) char lds[L1_SIZE];
  const int tid = threadIdx.x, lane = tid & 63, c = lane & 15, g = lane >> 4;
  const int w = __builtin_amdgcn_readfirstlane(tid >> 6);
  const int b = blockIdx.x, xcd = b & 7, slot = b >> 3;
  const int rb = (xcd >> 1) * 8 + (slot & 7), hg = (xcd & 1) * 4 + (slot >> 3);

  if (w < 3) {
    const rsrc_t rw = mkrs(wqkv, 512 * 1536 * 2);
    const int voff = (8 * g * 1536 + w * 512 + hg * 64 + 4 * c) * 2;
    u32x2 wb[PD1][8];
#pragma unroll
    for (int s = 0; s < PD1; ++s)
#pragma unroll
      for (int i = 0; i < 8; ++i) wb[s][i] = ld8(rw, voff, (32 * s + i) * 3072);
    f32x4 acc[4][4];
#pragma unroll
    for (int j = 0; j < 4; ++j)
#pragma unroll
      for (int t = 0; t < 4; ++t) acc[j][t] = f32x4{0.f, 0.f, 0.f, 0.f};
    int xa[4];
#pragma unroll
    for (int t = 0; t < 4; ++t) xa[t] = c * 256 + (((4 * t + g) ^ c) << 4);
    SCHED_FENCE();
    RAW_BARRIER();
#pragma unroll
    for (int s = 0; s < 16; ++s) {
      if (s == 4) RAW_BARRIER();
      u32x4 xf[4], wf[4];
#pragma unroll
      for (int t = 0; t < 4; ++t) xf[t] = *(const u32x4*)(lds + L1_X + (s >> 2) * 16384 + t * 4096 + xa[s & 3]);
      wperm(wb[s % PD1], wf);
      if (s + PD1 < 16) {
#pragma unroll
        for (int i = 0; i < 8; ++i) wb[s % PD1][i] = ld8(rw, voff, (32 * (s + PD1) + i) * 3072);
      }
#pragma unroll
      for (int j = 0; j < 4; ++j)
#pragma unroll
        for (int t = 0; t < 4; ++t) acc[j][t] = mfma(wf[j], xf[t], acc[j][t]);
      SCHED_FENCE();
    }
#pragma unroll
    for (int t = 0; t < 4; ++t)
#pragma unroll
      for (int h = 0; h < 2; ++h) {
        u32x4 v = {pk2(acc[0][t][2 * h], acc[1][t][2 * h]), pk2(acc[2][t][2 * h], acc[3][t][2 * h]),
                   pk2(acc[0][t][2 * h + 1], acc[1][t][2 * h + 1]), pk2(acc[2][t][2 * h + 1], acc[3][t][2 * h + 1])};
        *(u32x4*)(lds + L1_T + ((w * 4 + t) * 2 + h) * HS + lane * 16) = v;
      }
  } else {
    if (w >= 4) {
#pragma unroll
      for (int kq = 0; kq < 4; ++kq)
#pragma unroll
        for (int i = 0; i < 4; ++i) {
          const int pg = (w - 4) * 4 + i, p = 4 * pg + g;
          const int row = rb * 64 + 4 * (p & 15) + (p >> 4);
          const u16* src = x + row * 512 + (16 * kq + (c ^ (p & 15))) * 8;
          __builtin_amdgcn_global_load_lds((const u32*)src, (__attribute__((address_space(3))) u32*)(lds + L1_X + kq * 16384 + pg * 1024), 16, 0, 0);
        }
      asm volatile("s_waitcnt vmcnt(12)" ::: "memory");
      RAW_BARRIER();
      asm volatile("s_waitcnt vmcnt(0)" ::: "memory");
      RAW_BARRIER();
    } else {
      RAW_BARRIER();
      RAW_BARRIER();
    }
    if (w == 4) {
      const rsrc_t rp = mkrs(wphi, 32 * 256 * 2);
      const int voff = ((4 * hg + g) * 256 + 4 * (c & 3)) * 2;
      u32x2 wp[16];
#pragma unroll
      for (int d = 0; d < 16; ++d) wp[d] = ld8(rp, voff + d * 32, 0);
      const bool on = (g == (c >> 2));
#pragma unroll
      for (int h = 0; h < 2; ++h) {
        u32x4 f[4];
        wperm(wp + 8 * h, f);
#pragma unroll
        for (int jj = 0; jj < 4; ++jj) {
          u32x4 v = on ? f[jj] : u32x4{0u, 0u, 0u, 0u};
          *(u32x4*)(lds + L1_WF + (jj * 2 + h) * 1024 + lane * 16) = v;
        }
      }
    } else if ((w & 3) == 3) {
      const int hf = w >> 2;
      const u32x4 gv = *(const u32x4*)(gw + lane * 8);
      float xv[4][8], sum[4], gs[4];
#pragma unroll
      for (int q = 0; q < 4; ++q) {
        const int rr = 8 * hg + 4 * hf + q, p = 16 * (rr & 3) + (rr >> 2);
        const u32x4 v = *(const u32x4*)(lds + L1_X + g * 16384 + p * 256 + ((c ^ (p & 15)) << 4));
        float s = 0.f, t = 0.f;
#pragma unroll
        for (int e = 0; e < 4; ++e) {
          xv[q][2 * e] = blo(v[e]); xv[q][2 * e + 1] = bhi(v[e]);
          s += xv[q][2 * e] + xv[q][2 * e + 1];
          const u32 pr = pk2(xv[q][2 * e] * blo(gv[e]), xv[q][2 * e + 1] * bhi(gv[e]));
          t += blo(pr) + bhi(pr);
        }
        sum[q] = s; gs[q] = t;
      }
      float var[4];
#pragma unroll
      for (int q = 0; q < 4; ++q) { sum[q] = wave_sum(sum[q]) * (1.f / 512.f); gs[q] = wave_sum(gs[q]); }
#pragma unroll
      for (int q = 0; q < 4; ++q) {
        float s = 0.f;
#pragma unroll
        for (int e = 0; e < 8; ++e) { const float d = xv[q][e] - sum[q]; s += d * d; }
        var[q] = wave_sum(s) * (1.f / 512.f);
      }
#pragma unroll
      for (int q = 0; q < 4; ++q) {
        const float m = rbf(sum[q]);
        const float rs = 1.f / rbf(sqrtf(rbf(rbf(var[q]) + 1e-5f)));
#pragma unroll
        for (int e = 0; e < 4; ++e) {
          const u32 pr = pk2(xv[q][2 * e] - m, xv[q][2 * e + 1] - m);
          xv[q][2 * e] = blo(pr) * rs; xv[q][2 * e + 1] = bhi(pr) * rs;
        }
      }
      char* hp = ws + WS_H + (size_t)(rb * 8 + hg) * 8192 + (64 * (lane >> 3) + 2 * (lane & 7)) * 16 + hf * 8;
#pragma unroll
      for (int e = 0; e < 8; ++e)
        *(u32x2*)(hp + (16 * (e & 3) + (e >> 2)) * 16) = u32x2{pk2(xv[0][e], xv[1][e]), pk2(xv[2][e], xv[3][e])};
      if (lane == 0) {
        f32x4 o;
#pragma unroll
        for (int q = 0; q < 4; ++q) o[q] = rbf(1.f / (1.f + __expf(-rbf(gs[q]))));
        *(f32x4*)(ws + WS_G + (rb * 64 + 8 * hg + 4 * hf) * 4) = o;
      }
    }
  }
  RAW_BARRIER();
  {
    const int mat = w >> 2, tau = w & 3;
    const u32x4 b0 = *(const u32x4*)(lds + L1_T + ((mat * 4 + tau) * 2) * HS + lane * 16);
    const u32x4 b1 = *(const u32x4*)(lds + L1_T + ((mat * 4 + tau) * 2 + 1) * HS + lane * 16);
    float p[16];
#pragma unroll
    for (int jj = 0; jj < 4; ++jj) {
      const u32x4 a0 = *(const u32x4*)(lds + L1_WF + (jj * 2) * 1024 + lane * 16);
      const u32x4 a1 = *(const u32x4*)(lds + L1_WF + (jj * 2 + 1) * 1024 + lane * 16);
      f32x4 r = mfma(a1, b1, mfma(a0, b0, f32x4{0.f, 0.f, 0.f, 0.f}));
      const u32 u0 = pk2(r[0], r[1]), u1 = pk2(r[2], r[3]);
      p[jj] = blo(u0); p[4 + jj] = bhi(u0); p[8 + jj] = blo(u1); p[12 + jj] = bhi(u1);
    }
    float mx = p[0], mn = p[0];
#pragma unroll
    for (int f = 1; f < 16; ++f) { mx = fmaxf(mx, p[f]); mn = fminf(mn, p[f]); }
    const float L2E = 1.4426950408889634f;
    float ep[16], en[16], sp = 0.f, sn = 0.f;
#pragma unroll
    for (int f = 0; f < 16; ++f) {
      ep[f] = __builtin_amdgcn_exp2f((p[f] - mx) * L2E);
      en[f] = __builtin_amdgcn_exp2f((mn - p[f]) * L2E);
      sp += ep[f]; sn += en[f];
    }
    const float rp = __builtin_amdgcn_rcpf(sp), rn = __builtin_amdgcn_rcpf(sn);
    u32x4 ph[4];
#pragma unroll
    for (int t = 0; t < 4; ++t) {
      ph[0][t] = pk2(ep[2 * t] * rp, ep[2 * t + 1] * rp);
      ph[1][t] = pk2(ep[8 + 2 * t] * rp, ep[9 + 2 * t] * rp);
      ph[2][t] = pk2(en[2 * t] * rn, en[2 * t + 1] * rn);
      ph[3][t] = pk2(en[8 + 2 * t] * rn, en[9 + 2 * t] * rn);
    }
    if (mat == 0) {
      char* dst = ws + WS_PHIQ + ((size_t)(4 * hg + g) * 2048 + rb * 64 + 16 * tau + c) * 64;
#pragma unroll
      for (int pc = 0; pc < 4; ++pc) *(u32x4*)(dst + pc * 16) = ph[pc];
    } else {
#pragma unroll
      for (int pc = 0; pc < 4; ++pc) *(u32x4*)(lds + L1_PH + (tau * 4 + pc) * HS + lane * 16) = ph[pc];
    }
  }
  RAW_BARRIER();
  {
    const int hh = w & 3, sign = w >> 2;
    const int q = (lane >> 2) & 3, pp = lane & 3;
    const int ro = (pp >> 1) * HS + (16 * hh + 4 * g + q) * 16 + (pp & 1) * 8;
    f32x4 kv = {0.f, 0.f, 0.f, 0.f}, z = {0.f, 0.f, 0.f, 0.f};
    const u32x4 ones = {0x3f803f80u, 0x3f803f80u, 0x3f803f80u, 0x3f803f80u};
#pragma unroll
    for (int sg = 0; sg < 2; ++sg) {
      const u32x2 a0 = trread(lds + L1_PH + ((2 * sg) * 4 + 2 * sign) * HS + ro);
      const u32x2 a1 = trread(lds + L1_PH + ((2 * sg + 1) * 4 + 2 * sign) * HS + ro);
      const u32x2 v0 = trread(lds + L1_T + ((8 + 2 * sg) * 2) * HS + ro);
      const u32x2 v1 = trread(lds + L1_T + ((8 + 2 * sg + 1) * 2) * HS + ro);
      const u32x4 a = {a0.x, a0.y, a1.x, a1.y}, v = {v0.x, v0.y, v1.x, v1.y};
      kv = mfma(a, v, kv);
      z = mfma(a, ones, z);
    }
    const int hb = (4 * hg + hh) * 32 + rb;
    *(f32x4*)(ws + WS_KVP + ((size_t)hb * 512 + sign * 256 + lane * 4) * 4) = kv;
    if (c == 0) *(f32x4*)(ws + WS_ZP + ((size_t)hb * 32 + sign * 16 + 4 * g) * 4) = z;
  }
}

constexpr int PD = 5;
struct Stage { u32x2 w[8]; u32x4 p[4]; };
DEVI void load_stage(Stage& st, rsrc_t rw, int vw, int sw, int ldw, rsrc_t rp, int vp, int sp) {
#pragma unroll
  for (int i = 0; i < 8; ++i) st.w[i] = ld8(rw, vw, sw + i * ldw);
#pragma unroll
  for (int i = 0; i < 4; ++i) st.p[i] = ld16(rp, vp + i * 256, sp);
}

__global__ __launch_bounds__(256, 1) void k2(const u16* __restrict__ w1, const u16* __restrict__ b1, char* __restrict__ ws) {
  __shared__ __attribute__((aligned(16))) char lds[65536];
  const int tid = threadIdx.x, lane = tid & 63, c = lane & 15, g = lane >> 4;
  const int w = __builtin_amdgcn_readfirstlane(tid >> 6);
  const int b = blockIdx.x, xcd = b & 7, slot = b >> 3;
  if (slot < 28) {
    const int D0 = ((xcd & 1) * 4 + (slot & 3)) * 64, F0 = ((xcd >> 1) * 7 + (slot >> 2)) * 64;
    const rsrc_t rw = mkrs(w1, S_ * F_ * 2), rp = mkrs(ws + WS_H, 2097152);
    const int vw = (8 * g * F_ + F0 + 4 * c) * 2, vp = (g * 512 + D0 + c) * 16;
    constexpr int NS = 16;
    const int sw0 = w * NS * 32 * F_ * 2, sp0 = w * NS * 32768;
    Stage st[PD];
#pragma unroll
    for (int s = 0; s < PD; ++s) load_stage(st[s], rw, vw, sw0 + s * 32 * F_ * 2, F_ * 2, rp, vp, sp0 + s * 32768);
    f32x4 acc[4][4];
#pragma unroll
    for (int j = 0; j < 4; ++j)
#pragma unroll
      for (int i = 0; i < 4; ++i) acc[j][i] = f32x4{0.f, 0.f, 0.f, 0.f};
    SCHED_FENCE();
#pragma unroll
    for (int s = 0; s < NS; ++s) {
      u32x4 wf[4], pf[4];
      wperm(st[s % PD].w, wf);
#pragma unroll
      for (int i = 0; i < 4; ++i) pf[i] = st[s % PD].p[i];
#pragma unroll
      for (int i = 0; i < 4; ++i) acc[0][i] = mfma(wf[0], pf[i], acc[0][i]);
      if (s + PD < NS) load_stage(st[s % PD], rw, vw, sw0 + (s + PD) * 32 * F_ * 2, F_ * 2, rp, vp, sp0 + (s + PD) * 32768);
#pragma unroll
      for (int j = 1; j < 4; ++j)
#pragma unroll
        for (int i = 0; i < 4; ++i) acc[j][i] = mfma(wf[j], pf[i], acc[j][i]);
      SCHED_FENCE();
    }
    const u32x4 bv0 = *(const u32x4*)(b1 + F0 + 16 * g), bv1 = *(const u32x4*)(b1 + F0 + 16 * g + 8);
#pragma unroll
    for (int i = 0; i < 4; ++i)
#pragma unroll
      for (int j = 0; j < 4; ++j) *(f32x4*)(lds + ((i * 4 + w) * 4 + j) * 1024 + lane * 16) = acc[j][i];
    RAW_BARRIER();
    f32x4 r[4];
#pragma unroll
    for (int j = 0; j < 4; ++j) {
      r[j] = *(const f32x4*)(lds + ((w * 4 + 0) * 4 + j) * 1024 + lane * 16);
#pragma unroll
      for (int sw = 1; sw < 4; ++sw) r[j] += *(const f32x4*)(lds + ((w * 4 + sw) * 4 + j) * 1024 + lane * 16);
    }
    u32x4 o[2];
#pragma unroll
    for (int t = 0; t < 8; ++t) {
      const u32 bb = t < 4 ? bv0[t & 3] : bv1[t & 3];
      const u32 pr = pk2(r[(2 * t) & 3][t >> 1], r[(2 * t + 1) & 3][t >> 1]);
      const u32 ps = pk2(blo(pr) + blo(bb), bhi(pr) + bhi(bb));
      const float u0 = blo(ps), u1 = bhi(ps);
      const float k0 = -2.302208198f, k1c = -0.1029432397f;
      const float e0 = __builtin_amdgcn_exp2f(u0 * (k0 + k1c * u0 * u0));
      const float e1 = __builtin_amdgcn_exp2f(u1 * (k0 + k1c * u1 * u1));
      o[t >> 2][t & 3] = pk2(u0 * __builtin_amdgcn_rcpf(1.f + e0), u1 * __builtin_amdgcn_rcpf(1.f + e1));
    }
    char* up = ws + WS_U + ((size_t)(F0 / 8 + 2 * g) * 512 + D0 + 16 * w + c) * 16;
    *(u32x4*)up = o[0];
    *(u32x4*)(up + 8192) = o[1];
  } else {
    const int h = xcd * 4 + (slot - 28);
    const rsrc_t rk = mkrs(ws + WS_KVP + (size_t)h * 65536, 65536), rq = mkrs(ws + WS_PHIQ + (size_t)h * 131072, 131072);
    u32x4 kp[16];
#pragma unroll
    for (int n = 0; n < 16; ++n) kp[n] = ld16(rk, (tid >> 7) * 32768 + (tid & 127) * 16, n * 2048);
    const f32x4 zp = *(const f32x4*)(ws + WS_ZP + (size_t)h * 4096 + tid * 16);
    u32x4 ph[32];
#pragma unroll
    for (int n = 0; n < 32; ++n) ph[n] = ld16(rq, c * 64 + g * 16, (w * 32 + n) * 1024);
    f32x4 s = __builtin_bit_cast(f32x4, kp[0]);
#pragma unroll
    for (int n = 1; n < 16; ++n) s += __builtin_bit_cast(f32x4, kp[n]);
    *(f32x4*)(lds + tid * 16) = s;
    *(f32x4*)(lds + 4096 + tid * 16) = zp;
    RAW_BARRIER();
    if (tid < 32) {
      float t = 0.f;
#pragma unroll
      for (int n = 0; n < 32; ++n) t += *(const float*)(lds + 4096 + n * 128 + tid * 4);
      *(float*)(lds + 8192 + tid * 4) = t;
    }
    RAW_BARRIER();
    u32x4 akv, az;
    {
      const int o0 = ((g >> 1) * 256 + 2 * (g & 1) * 64 + c * 4) * 4;
      const f32x4 k0 = *(const f32x4*)(lds + o0) + *(const f32x4*)(lds + 2048 + o0);
      const f32x4 k1v = *(const f32x4*)(lds + o0 + 256) + *(const f32x4*)(lds + 2048 + o0 + 256);
      akv = u32x4{pk2(k0[0], k0[1]), pk2(k0[2], k0[3]), pk2(k1v[0], k1v[1]), pk2(k1v[2], k1v[3])};
      const f32x4 z0 = *(const f32x4*)(lds + 8192 + g * 32), z1 = *(const f32x4*)(lds + 8192 + g * 32 + 16);
      az = u32x4{pk2(z0[0], z0[1]), pk2(z0[2], z0[3]), pk2(z1[0], z1[1]), pk2(z1[2], z1[3])};
    }
    char* op = ws + WS_O + ((size_t)(2 * h + (g >> 1)) * 2048 + w * 512 + c) * 16 + (g & 1) * 8;
#pragma unroll
    for (int n = 0; n < 32; ++n) {
      const f32x4 num = mfma(akv, ph[n], f32x4{0.f, 0.f, 0.f, 0.f});
      const f32x4 den = mfma(az, ph[n], f32x4{0.f, 0.f, 0.f, 0.f});
      const float rd = 1.f / rbf(rbf(den[0]) + 1e-6f);
      const u32 n0 = pk2(num[0], num[1]), n1 = pk2(num[2], num[3]);
      *(u32x2*)(op + n * 256) = u32x2{pk2(blo(n0) * rd, bhi(n0) * rd), pk2(blo(n1) * rd, bhi(n1) * rd)};
    }
  }
}

__global__ __launch_bounds__(256, 1) void k3(const u16* __restrict__ w2, const u16* __restrict__ wo, const u16* __restrict__ b2,
                                              const char* __restrict__ ws, u16* __restrict__ out) {
  __shared__ __attribute__((aligned(16))) char lds[65536];
  const int tid = threadIdx.x, lane = tid & 63, c = lane & 15, g = lane >> 4;
  const int w = __builtin_amdgcn_readfirstlane(tid >> 6);
  const int b = blockIdx.x, xcd = b & 7, slot = b >> 3;
  const int S0 = (xcd * 4 + (slot & 3)) * 64, D0 = (slot >> 2) * 64;
  const rsrc_t rw2 = mkrs(w2, F_ * S_ * 2), ru = mkrs(ws + WS_U, 1835008);
  const rsrc_t rwo = mkrs(wo, D_ * D_ * 2), ro = mkrs(ws + WS_O, 2097152);
  const int vw2 = (8 * g * S_ + S0 + 4 * c) * 2, vu = (g * 512 + D0 + c) * 16;
  const int vwo = (8 * g * D_ + D0 + 4 * c) * 2, vo = (g * 2048 + S0 + c) * 16;
  constexpr int NY = 14, NA = 4, NS = NY + NA;
  const int sw2 = w * NY * 32 * S_ * 2, su = w * NY * 32768, swo = w * NA * 32 * D_ * 2, so = w * NA * 131072;
  Stage st[PD];
  auto issue = [&](int s) {
    if (s < NY) load_stage(st[s % PD], rw2, vw2, sw2 + s * 32 * S_ * 2, S_ * 2, ru, vu, su + s * 32768);
    else load_stage(st[s % PD], rwo, vwo, swo + (s - NY) * 32 * D_ * 2, D_ * 2, ro, vo, so + (s - NY) * 131072);
  };
#pragma unroll
  for (int s = 0; s < PD; ++s) issue(s);
  f32x4 ay[4][4], aa[4][4];
#pragma unroll
  for (int j = 0; j < 4; ++j)
#pragma unroll
    for (int i = 0; i < 4; ++i) { ay[j][i] = f32x4{0.f, 0.f, 0.f, 0.f}; aa[j][i] = f32x4{0.f, 0.f, 0.f, 0.f}; }
  SCHED_FENCE();
  f32x4 gv[4];
  float gown[4], bown[4];
#pragma unroll
  for (int s = 0; s < NS; ++s) {
    u32x4 wf[4], pf[4];
    wperm(st[s % PD].w, wf);
#pragma unroll
    for (int i = 0; i < 4; ++i) pf[i] = st[s % PD].p[i];
#pragma unroll
    for (int i = 0; i < 4; ++i) {
      if (s < NY) ay[0][i] = mfma(wf[0], pf[i], ay[0][i]);
      else aa[i][0] = mfma(pf[i], wf[0], aa[i][0]);
    }
    if (s + PD < NS) issue(s + PD);
    if (s + PD == NS) {
#pragma unroll
      for (int r = 0; r < 4; ++r) {
        gv[r] = *(const f32x4*)(ws + WS_G + (S0 + 16 * g + 4 * r) * 4);
        gown[r] = *(const float*)(ws + WS_G + (S0 + 16 * g + 4 * r + w) * 4);
        bown[r] = blo(b2[S0 + 16 * g + 4 * r + w]);
      }
    }
#pragma unroll
    for (int j = 1; j < 4; ++j)
#pragma unroll
      for (int i = 0; i < 4; ++i) {
        if (s < NY) ay[j][i] = mfma(wf[j], pf[i], ay[j][i]);
        else aa[i][j] = mfma(pf[i], wf[j], aa[i][j]);
      }
    SCHED_FENCE();
  }
#pragma unroll
  for (int j = 0; j < 4; ++j) {
    float ga[4], gb[4];
#pragma unroll
    for (int r = 0; r < 4; ++r) { ga[r] = gv[r][j]; gb[r] = rbf(1.f - ga[r]); }
#pragma unroll
    for (int i = 0; i < 4; ++i) {
      f32x4 v;
#pragma unroll
      for (int r = 0; r < 4; ++r) v[r] = ga[r] * aa[j][i][r] + gb[r] * ay[j][i][r];
      *(f32x4*)(lds + ((j * 4 + w) * 4 + i) * 1024 + lane * 16) = v;
    }
  }
  RAW_BARRIER();
  f32x4 r4[4];
#pragma unroll
  for (int i = 0; i < 4; ++i) {
    r4[i] = *(const f32x4*)(lds + ((w * 4 + 0) * 4 + i) * 1024 + lane * 16);
#pragma unroll
    for (int sw = 1; sw < 4; ++sw) r4[i] += *(const f32x4*)(lds + ((w * 4 + sw) * 4 + i) * 1024 + lane * 16);
  }
#pragma unroll
  for (int r = 0; r < 4; ++r) {
    const float t = rbf(1.f - gown[r]) * bown[r];
    *(u32x2*)(out + (size_t)(S0 + 16 * g + 4 * r + w) * 512 + D0 + 4 * c) =
        u32x2{pk2(r4[0][r] + t, r4[1][r] + t), pk2(r4[2][r] + t, r4[3][r] + t)};
  }
}

extern "C" void kernel_launch(void* const* d_in, const int* in_sizes, int n_in, void* d_out, int out_size, void* d_ws,
                              size_t ws_size, hipStream_t stream) {
  const u16* x = (const u16*)d_in[0];
  const u16* wqkv = (const u16*)d_in[1];
  const u16* wphi = (const u16*)d_in[2];
  const u16* wo = (const u16*)d_in[3];
  const u16* w1 = (const u16*)d_in[5];
  const u16* b1 = (const u16*)d_in[6];
  const u16* w2 = (const u16*)d_in[7];
  const u16* b2 = (const u16*)d_in[8];
  const u16* gw = (const u16*)d_in[9];
  char* ws = (char*)d_ws;
  k1<<<256, 512, 0, stream>>>(x, wqkv, wphi, gw, ws);
  k2<<<256, 256, 0, stream>>>(w1, b1, ws);
  k3<<<256, 256, 0, stream>>>(w2, wo, b2, ws, (u16*)d_out);
}
```

```cpp
#include <hip/hip_runtime.h>
#include <hip/hip_bf16.h>
#include <type_traits>

typedef unsigned short u16;
typedef unsigned int u32;
typedef __attribute__((ext_vector_type(8))) __bf16 bf16x8;
typedef __attribute__((ext_vector_type(2))) __bf16 bf16x2;
typedef __attribute__((ext_vector_type(4))) short s16x4;
typedef __attribute__((ext_vector_type(4))) float f32x4;
typedef __attribute__((ext_vector_type(2))) float f32x2;
typedef __attribute__((ext_vector_type(4))) unsigned int u32x4;
typedef __attribute__((ext_vector_type(2))) unsigned int u32x2;
typedef __amdgpu_buffer_rsrc_t rsrc_t;

#define DEVI __device__ __forceinline__
#define SCHED_FENCE() __builtin_amdgcn_sched_barrier(0)
#define RAW_BARRIER() do { asm volatile("s_waitcnt lgkmcnt(0)" ::: "memory"); __builtin_amdgcn_s_barrier(); asm volatile("" ::: "memory"); } while (0)

#define STAMP(k, s)
#define STAMP_DECL
#define STAMP_OUT(k)
constexpr int S_ = 2048, D_ = 512, F_ = 1792;
constexpr size_t WS_H = 0;
constexpr size_t WS_PHIQ = WS_H + 2097152;
constexpr size_t WS_KVP = WS_PHIQ + 4194304;
constexpr size_t WS_ZP = WS_KVP + 2097152;
constexpr size_t WS_G = WS_ZP + 131072;
constexpr size_t WS_U = WS_G + 8192;
constexpr size_t WS_O = WS_U + 1835008;

DEVI rsrc_t mkrs(const void* p, int bytes) { return __builtin_amdgcn_make_buffer_rsrc((void*)p, (short)0, bytes, 0x00020000); }
DEVI u32x4 ld16(rsrc_t r, int v, int s) { return __builtin_amdgcn_raw_buffer_load_b128(r, v, s, 0); }
DEVI u32x2 ld8(rsrc_t r, int v, int s) { return __builtin_amdgcn_raw_buffer_load_b64(r, v, s, 0); }
DEVI u32 pk2(float lo, float hi) { f32x2 v = {lo, hi}; return __builtin_bit_cast(u32, __builtin_convertvector(v, bf16x2)); }
DEVI float blo(u32 v) { return __builtin_bit_cast(float, v << 16); }
DEVI float bhi(u32 v) { return __builtin_bit_cast(float, v & 0xffff0000u); }
DEVI float rbf(float x) { return blo(pk2(x, x)); }
DEVI f32x4 mfma(u32x4 a, u32x4 b, f32x4 c) {
  return __builtin_amdgcn_mfma_f32_16x16x32_bf16(__builtin_bit_cast(bf16x8, a), __builtin_bit_cast(bf16x8, b), c, 0, 0, 0);
}
DEVI void wperm(const u32x2* w, u32x4* f) {
#pragma unroll
  for (int t = 0; t < 4; ++t) {
    f[0][t] = __builtin_amdgcn_perm(w[2 * t + 1].x, w[2 * t].x, 0x05040100u);
    f[1][t] = __builtin_amdgcn_perm(w[2 * t + 1].x, w[2 * t].x, 0x07060302u);
    f[2][t] = __builtin_amdgcn_perm(w[2 * t + 1].y, w[2 * t].y, 0x05040100u);
    f[3][t] = __builtin_amdgcn_perm(w[2 * t + 1].y, w[2 * t].y, 0x07060302u);
  }
}
template <int CTRL> DEVI float dppf(float x) {
  return __builtin_bit_cast(float, __builtin_amdgcn_update_dpp(0, __builtin_bit_cast(int, x), CTRL, 0xf, 0xf, true));
}
DEVI float wave_sum(float x) {
  x += dppf<0xB1>(x); x += dppf<0x4E>(x); x += dppf<0x141>(x); x += dppf<0x140>(x);
  u32 b = __builtin_bit_cast(u32, x);
  auto r = __builtin_amdgcn_permlane16_swap(b, b, false, false);
  x = __builtin_bit_cast(float, (u32)r[0]) + __builtin_bit_cast(float, (u32)r[1]);
  b = __builtin_bit_cast(u32, x);
  auto q = __builtin_amdgcn_permlane32_swap(b, b, false, false);
  return __builtin_bit_cast(float, (u32)q[0]) + __builtin_bit_cast(float, (u32)q[1]);
}
DEVI u32x2 trread(const char* p) {
  s16x4 v = __builtin_amdgcn_ds_read_tr16_b64_v4i16((__attribute__((address_space(3))) s16x4*)(p));
  return __builtin_bit_cast(u32x2, v);
}

typedef __attribute__((address_space(3))) char lchar;
DEVI u32 ldsaddr(const char* p) { return (u32)(size_t)(const lchar*)p; }
template <int N> DEVI void wait_vm() { asm volatile("s_waitcnt vmcnt(%0)" ::"n"(N) : "memory"); }
DEVI void dma_stage(char* dst, rsrc_t r, int ve, int vo, int so, int ld) {
#pragma unroll
  for (int n = 0; n < 4; ++n)
    __builtin_amdgcn_raw_ptr_buffer_load_lds(r, (lchar*)(dst + n * 1024), 16, (n & 1) ? vo : ve, so + n * 8 * ld, 0, 0);
}
DEVI int dma_voff(int lane, int ld, int odd) { return (lane >> 3) * ld + (((lane & 7) ^ ((((lane >> 4) & 1) | (odd << 1)) << 1)) << 4); }
DEVI void tr_addr(u32* ta, u32 base, int lane) {
  const int kg = lane >> 4, q = (lane >> 2) & 3, pp = lane & 3;
  const int fr = ((q >> 1) & 1) | ((kg & 1) << 1);
#pragma unroll
  for (int j = 0; j < 4; ++j) ta[j] = base + (8 * kg + q) * 128 + (((2 * j + (pp >> 1)) ^ (fr << 1)) << 4) + (pp & 1) * 8;
}
struct TrFrag { u32x2 h[8]; };
template <int OFF> DEVI void tr_issue(TrFrag& t, const u32* ta) {
  asm volatile(
      "ds_read_b64_tr_b16 %0, %8 offset:%12\n\tds_read_b64_tr_b16 %1, %8 offset:%13\n\t"
      "ds_read_b64_tr_b16 %2, %9 offset:%12\n\tds_read_b64_tr_b16 %3, %9 offset:%13\n\t"
      "ds_read_b64_tr_b16 %4, %10 offset:%12\n\tds_read_b64_tr_b16 %5, %10 offset:%13\n\t"
      "ds_read_b64_tr_b16 %6, %11 offset:%12\n\tds_read_b64_tr_b16 %7, %11 offset:%13"
      : "=&v"(t.h[0]), "=&v"(t.h[1]), "=&v"(t.h[2]), "=&v"(t.h[3]), "=&v"(t.h[4]), "=&v"(t.h[5]), "=&v"(t.h[6]), "=&v"(t.h[7])
      : "v"(ta[0]), "v"(ta[1]), "v"(ta[2]), "v"(ta[3]), "n"(OFF), "n"(OFF + 512)
      : "memory");
}
DEVI void tr_wait(TrFrag& t, u32x4* f) {
  asm volatile("s_waitcnt lgkmcnt(0)"
               : "+v"(t.h[0]), "+v"(t.h[1]), "+v"(t.h[2]), "+v"(t.h[3]), "+v"(t.h[4]), "+v"(t.h[5]), "+v"(t.h[6]), "+v"(t.h[7])
               :: "memory");
#pragma unroll
  for (int j = 0; j < 4; ++j) f[j] = u32x4{t.h[2 * j].x, t.h[2 * j].y, t.h[2 * j + 1].x, t.h[2 * j + 1].y};
}
template <int OFF> DEVI void x_issue(u32x4* xf, u32 a) {
  asm volatile("ds_read_b128 %0, %4 offset:%5\n\tds_read_b128 %1, %4 offset:%6\n\tds_read_b128 %2, %4 offset:%7\n\tds_read_b128 %3, %4 offset:%8"
               : "=&v"(xf[0]), "=&v"(xf[1]), "=&v"(xf[2]), "=&v"(xf[3])
               : "v"(a), "n"(OFF), "n"(OFF + 4096), "n"(OFF + 8192), "n"(OFF + 12288) : "memory");
}
template <int I, int N, class Fn> DEVI void static_for(Fn&& f) {
  if constexpr (I < N) { f(std::integral_constant<int, I>{}); static_for<I + 1, N>(f); }
}
constexpr int imin(int a, int b) { return a < b ? a : b; }

constexpr int HS = 1152;
constexpr int L1_X = 0;
constexpr int PD1 = 7;
constexpr int L1_R = 65536;
constexpr int RS1 = PD1 * 4096;
constexpr int L1_PH = L1_X;
constexpr int L1_WF = L1_R + 3 * RS1;
constexpr int L1_SIZE = L1_WF + 8192;

__global__ __launch_bounds__(512, 1) void k1(const u16* __restrict__ x, const u16* __restrict__ wqkv,
                                              const u16* __restrict__ wphi, const u16* __restrict__ gw,
                                              char* __restrict__ ws) {
  __shared__ __attribute__((aligned(16))) char lds[L1_SIZE];
  const int tid = threadIdx.x, lane = tid & 63, c = lane & 15, g = lane >> 4;
  const int w = __builtin_amdgcn_readfirstlane(tid >> 6);
  const int b = blockIdx.x, xcd = b & 7, slot = b >> 3;
  const int rb = (xcd >> 1) * 8 + (slot & 7), hg = (xcd & 1) * 4 + (slot >> 3);

  STAMP_DECL;
  STAMP(0, 0);
  if (w < 3) {
    const rsrc_t rw = mkrs(wqkv, 512 * 1536 * 2);
    const int cb = (w * 512 + hg * 64) * 2;
    const int ve = dma_voff(lane, 3072, 0) + cb, vo = dma_voff(lane, 3072, 1) + cb;
    char* ring = lds + L1_R + w * RS1;
#pragma unroll
    for (int s = 0; s < PD1; ++s) dma_stage(ring + s * 4096, rw, ve, vo, s * 32 * 3072, 3072);
    f32x4 acc[4][4];
#pragma unroll
    for (int j = 0; j < 4; ++j)
#pragma unroll
      for (int t = 0; t < 4; ++t) acc[j][t] = f32x4{0.f, 0.f, 0.f, 0.f};
    u32 xa[4], ta[4];
#pragma unroll
    for (int t = 0; t < 4; ++t) xa[t] = ldsaddr(lds) + c * 256 + (((4 * t + g) ^ c) << 4);
    tr_addr(ta, ldsaddr(ring), lane);
    STAMP(0, 1);
    RAW_BARRIER();
    STAMP(0, 2);
    TrFrag tf[2];
    u32x4 xf[2][4];
    wait_vm<4 * (PD1 - 1)>();
    tr_issue<0>(tf[0], ta);
    x_issue<0>(xf[0], xa[0]);
    static_for<0, 16>([&](auto sc) {
      constexpr int s = decltype(sc)::value;
      u32x4 wf[4];
      tr_wait(tf[s & 1], wf);
      asm volatile("" : "+v"(xf[s & 1][0]), "+v"(xf[s & 1][1]), "+v"(xf[s & 1][2]), "+v"(xf[s & 1][3]));
      if constexpr (s + PD1 < 16) dma_stage(ring + (s % PD1) * 4096, rw, ve, vo, (s + PD1) * 32 * 3072, 3072);
      if constexpr (s + 1 < 16) {
        if constexpr (s + 1 == 4) { __builtin_amdgcn_s_barrier(); STAMP(0, 3); }
        wait_vm<4 * imin(PD1 - 1, 14 - s)>();
        tr_issue<((s + 1) % PD1) * 4096>(tf[(s + 1) & 1], ta);
        x_issue<((s + 1) >> 2) * 16384>(xf[(s + 1) & 1], xa[(s + 1) & 3]);
      }
      SCHED_FENCE();
#pragma unroll
      for (int j = 0; j < 4; ++j)
#pragma unroll
        for (int t = 0; t < 4; ++t) acc[j][t] = mfma(wf[j], xf[s & 1][t], acc[j][t]);
      SCHED_FENCE();
    });
#pragma unroll
    for (int t = 0; t < 4; ++t)
#pragma unroll
      for (int j = 0; j < 4; ++j)
        *(u32x2*)(ring + (t * 2 + (g >> 1)) * HS + (16 * j + c) * 16 + (g & 1) * 8) =
            u32x2{pk2(acc[j][t][0], acc[j][t][1]), pk2(acc[j][t][2], acc[j][t][3])};
  } else {
    if (w >= 4) {
#pragma unroll
      for (int kq = 0; kq < 4; ++kq)
#pragma unroll
        for (int i = 0; i < 4; ++i) {
          const int pg = (w - 4) * 4 + i, p = 4 * pg + g;
          const int row = rb * 64 + p;
          const u16* src = x + row * 512 + (16 * kq + (c ^ (p & 15))) * 8;
          __builtin_amdgcn_global_load_lds((const u32*)src, (__attribute__((address_space(3))) u32*)(lds + L1_X + kq * 16384 + pg * 1024), 16, 0, 0);
        }
      asm volatile("s_waitcnt vmcnt(12)" ::: "memory");
      RAW_BARRIER();
      asm volatile("s_waitcnt vmcnt(0)" ::: "memory");
      RAW_BARRIER();
    } else {
      RAW_BARRIER();
      RAW_BARRIER();
    }
    if (w == 4) {
      const rsrc_t rp = mkrs(wphi, 32 * 256 * 2);
      const int voff = ((4 * hg + g) * 256 + 4 * (c & 3)) * 2;
      u32x2 wp[16];
#pragma unroll
      for (int d = 0; d < 16; ++d) wp[d] = ld8(rp, voff + d * 32, 0);
      const bool on = (g == (c >> 2));
#pragma unroll
      for (int h = 0; h < 2; ++h) {
        u32x4 f[4];
        wperm(wp + 8 * h, f);
#pragma unroll
        for (int jj = 0; jj < 4; ++jj) {
          u32x4 v = on ? f[jj] : u32x4{0u, 0u, 0u, 0u};
          *(u32x4*)(lds + L1_WF + (jj * 2 + h) * 1024 + lane * 16) = v;
        }
      }
    } else if ((w & 3) == 3) {
      const int hf = w >> 2;
      const u32x4 gv = *(const u32x4*)(gw + lane * 8);
      float xv[4][8], sum[4], gs[4];
#pragma unroll
      for (int q = 0; q < 4; ++q) {
        const int p = 8 * hg + 4 * hf + q;
        const u32x4 v = *(const u32x4*)(lds + L1_X + g * 16384 + p * 256 + ((c ^ (p & 15)) << 4));
        float s = 0.f, t = 0.f;
#pragma unroll
        for (int e = 0; e < 4; ++e) {
          xv[q][2 * e] = blo(v[e]); xv[q][2 * e + 1] = bhi(v[e]);
          s += xv[q][2 * e] + xv[q][2 * e + 1];
          const u32 pr = pk2(xv[q][2 * e] * blo(gv[e]), xv[q][2 * e + 1] * bhi(gv[e]));
          t += blo(pr) + bhi(pr);
        }
        sum[q] = s; gs[q] = t;
      }
      float var[4];
#pragma unroll
      for (int q = 0; q < 4; ++q) { sum[q] = wave_sum(sum[q]) * (1.f / 512.f); gs[q] = wave_sum(gs[q]); }
#pragma unroll
      for (int q = 0; q < 4; ++q) {
        float s = 0.f;
#pragma unroll
        for (int e = 0; e < 8; ++e) { const float d = xv[q][e] - sum[q]; s += d * d; }
        var[q] = wave_sum(s) * (1.f / 512.f);
      }
#pragma unroll
      for (int q = 0; q < 4; ++q) {
        const float m = rbf(sum[q]);
        const float rs = 1.f / rbf(sqrtf(rbf(rbf(var[q]) + 1e-5f)));
#pragma unroll
        for (int e = 0; e < 4; ++e) {
          const u32 pr = pk2(xv[q][2 * e] - m, xv[q][2 * e + 1] - m);
          xv[q][2 * e] = blo(pr) * rs; xv[q][2 * e + 1] = bhi(pr) * rs;
        }
      }
      char* hp = ws + WS_H + (size_t)(rb * 8 + hg) * 8192 + lane * 128 + hf * 8;
#pragma unroll
      for (int e = 0; e < 8; ++e)
        *(u32x2*)(hp + e * 16) = u32x2{pk2(xv[0][e], xv[1][e]), pk2(xv[2][e], xv[3][e])};
      if (lane == 0) {
        f32x4 o;
#pragma unroll
        for (int q = 0; q < 4; ++q) o[q] = rbf(1.f / (1.f + __expf(-rbf(gs[q]))));
        *(f32x4*)(ws + WS_G + (rb * 64 + 8 * hg + 4 * hf) * 4) = o;
      }
    }
  }
  STAMP(0, 4);
  RAW_BARRIER();
  STAMP(0, 5);
  {
    const int mat = w >> 2, tau = w & 3;
    const u32x4 b0 = *(const u32x4*)(lds + L1_R + mat * RS1 + (tau * 2) * HS + lane * 16);
    const u32x4 b1 = *(const u32x4*)(lds + L1_R + mat * RS1 + (tau * 2 + 1) * HS + lane * 16);
    float p[16];
#pragma unroll
    for (int jj = 0; jj < 4; ++jj) {
      const u32x4 a0 = *(const u32x4*)(lds + L1_WF + (jj * 2) * 1024 + lane * 16);
      const u32x4 a1 = *(const u32x4*)(lds + L1_WF + (jj * 2 + 1) * 1024 + lane * 16);
      f32x4 r = mfma(a1, b1, mfma(a0, b0, f32x4{0.f, 0.f, 0.f, 0.f}));
      const u32 u0 = pk2(r[0], r[1]), u1 = pk2(r[2], r[3]);
      p[jj] = blo(u0); p[4 + jj] = bhi(u0); p[8 + jj] = blo(u1); p[12 + jj] = bhi(u1);
    }
    float mx = p[0], mn = p[0];
#pragma unroll
    for (int f = 1; f < 16; ++f) { mx = fmaxf(mx, p[f]); mn = fminf(mn, p[f]); }
    const float L2E = 1.4426950408889634f;
    float ep[16], en[16], sp = 0.f, sn = 0.f;
#pragma unroll
    for (int f = 0; f < 16; ++f) {
      ep[f] = __builtin_amdgcn_exp2f((p[f] - mx) * L2E);
      en[f] = __builtin_amdgcn_exp2f((mn - p[f]) * L2E);
      sp += ep[f]; sn += en[f];
    }
    const float rp = __builtin_amdgcn_rcpf(sp), rn = __builtin_amdgcn_rcpf(sn);
    u32x4 ph[4];
#pragma unroll
    for (int t = 0; t < 4; ++t) {
      ph[0][t] = pk2(ep[2 * t] * rp, ep[2 * t + 1] * rp);
      ph[1][t] = pk2(ep[8 + 2 * t] * rp, ep[9 + 2 * t] * rp);
      ph[2][t] = pk2(en[2 * t] * rn, en[2 * t + 1] * rn);
      ph[3][t] = pk2(en[8 + 2 * t] * rn, en[9 + 2 * t] * rn);
    }
    if (mat == 0) {
      char* dst = ws + WS_PHIQ + ((size_t)(4 * hg + g) * 2048 + rb * 64 + 16 * tau + c) * 64;
#pragma unroll
      for (int pc = 0; pc < 4; ++pc) *(u32x4*)(dst + pc * 16) = ph[pc];
    } else {
#pragma unroll
      for (int pc = 0; pc < 4; ++pc) *(u32x4*)(lds + L1_PH + (tau * 4 + pc) * HS + lane * 16) = ph[pc];
    }
  }
  STAMP(0, 6);
  RAW_BARRIER();
  {
    const int hh = w & 3, sign = w >> 2;
    const int q = (lane >> 2) & 3, pp = lane & 3;
    const int ro = (pp >> 1) * HS + (16 * hh + 4 * g + q) * 16 + (pp & 1) * 8;
    f32x4 kv = {0.f, 0.f, 0.f, 0.f}, z = {0.f, 0.f, 0.f, 0.f};
    const u32x4 ones = {0x3f803f80u, 0x3f803f80u, 0x3f803f80u, 0x3f803f80u};
#pragma unroll
    for (int sg = 0; sg < 2; ++sg) {
      const u32x2 a0 = trread(lds + L1_PH + ((2 * sg) * 4 + 2 * sign) * HS + ro);
      const u32x2 a1 = trread(lds + L1_PH + ((2 * sg + 1) * 4 + 2 * sign) * HS + ro);
      const u32x2 v0 = trread(lds + L1_R + 2 * RS1 + ((2 * sg) * 2) * HS + ro);
      const u32x2 v1 = trread(lds + L1_R + 2 * RS1 + ((2 * sg + 1) * 2) * HS + ro);
      const u32x4 a = {a0.x, a0.y, a1.x, a1.y}, v = {v0.x, v0.y, v1.x, v1.y};
      kv = mfma(a, v, kv);
      z = mfma(a, ones, z);
    }
    const int hb = (4 * hg + hh) * 32 + rb;
    *(f32x4*)(ws + WS_KVP + ((size_t)hb * 512 + sign * 256 + lane * 4) * 4) = kv;
    if (c == 0) *(f32x4*)(ws + WS_ZP + ((size_t)hb * 32 + sign * 16 + 4 * g) * 4) = z;
  }
  STAMP(0, 7);
  STAMP_OUT(0);
}

constexpr int PD = 6;
constexpr int RS = PD * 4096;
DEVI void load_p(u32x4* p, rsrc_t rp, int vp, int sp) {
#pragma unroll
  for (int i = 0; i < 4; ++i) p[i] = ld16(rp, vp + i * 256, sp);
}

__global__ __launch_bounds__(256, 1) void k2(const u16* __restrict__ w1, const u16* __restrict__ b1, char* __restrict__ ws) {
  __shared__ __attribute__((aligned(16))) char lds[4 * RS];
  const int tid = threadIdx.x, lane = tid & 63, c = lane & 15, g = lane >> 4;
  const int w = __builtin_amdgcn_readfirstlane(tid >> 6);
  const int b = blockIdx.x, xcd = b & 7, slot = b >> 3;
  STAMP_DECL;
  STAMP(1, 0);
  if (slot < 28) {
    const int D0 = ((xcd & 1) * 4 + (slot & 3)) * 64, F0 = ((xcd >> 1) * 7 + (slot >> 2)) * 64;
    const rsrc_t rw = mkrs(w1, S_ * F_ * 2), rp = mkrs(ws + WS_H, 2097152);
    const int ve = dma_voff(lane, F_ * 2, 0) + F0 * 2, vo = dma_voff(lane, F_ * 2, 1) + F0 * 2, vp = (g * 512 + D0 + c) * 16;
    constexpr int NS = 16;
    const int sw0 = w * NS * 32 * F_ * 2, sp0 = w * NS * 32768;
    char* ring = lds + w * RS;
    u32x4 pb[PD][4];
#pragma unroll
    for (int s = 0; s < PD; ++s) {
      dma_stage(ring + s * 4096, rw, ve, vo, sw0 + s * 32 * F_ * 2, F_ * 2);
      load_p(pb[s], rp, vp, sp0 + s * 32768);
      SCHED_FENCE();
    }
    f32x4 acc[4][4];
#pragma unroll
    for (int j = 0; j < 4; ++j)
#pragma unroll
      for (int i = 0; i < 4; ++i) acc[j][i] = f32x4{0.f, 0.f, 0.f, 0.f};
    u32 ta[4];
    tr_addr(ta, ldsaddr(ring), lane);
    STAMP(1, 1);
    u32x4 bv0, bv1;
    static_for<0, NS>([&](auto sc) {
      constexpr int s = decltype(sc)::value;
      if (s == 1) STAMP(1, 2);
      if (s == 8) STAMP(1, 3);
      u32x4 wf[4], pf[4];
      TrFrag tf;
      wait_vm<4 + 8 * imin(PD - 1, NS - 1 - s)>();
      tr_issue<(s % PD) * 4096>(tf, ta);
      tr_wait(tf, wf);
#pragma unroll
      for (int i = 0; i < 4; ++i) pf[i] = pb[s % PD][i];
      if constexpr (s + PD < NS) {
        dma_stage(ring + (s % PD) * 4096, rw, ve, vo, sw0 + (s + PD) * 32 * F_ * 2, F_ * 2);
        load_p(pb[s % PD], rp, vp, sp0 + (s + PD) * 32768);
      }
      if constexpr (s == NS - 1) { bv0 = *(const u32x4*)(b1 + F0 + 16 * g); bv1 = *(const u32x4*)(b1 + F0 + 16 * g + 8); }
      SCHED_FENCE();
#pragma unroll
      for (int j = 0; j < 4; ++j)
#pragma unroll
        for (int i = 0; i < 4; ++i) acc[j][i] = mfma(wf[j], pf[i], acc[j][i]);
      SCHED_FENCE();
    });
    STAMP(1, 4);
#pragma unroll
    for (int i = 0; i < 4; ++i)
#pragma unroll
      for (int j = 0; j < 4; ++j) *(f32x4*)(ring + (i * 4 + j) * 1024 + lane * 16) = acc[j][i];
    RAW_BARRIER();
    f32x4 r[4];
#pragma unroll
    for (int q = 0; q < 4; ++q) {
      r[q] = *(const f32x4*)(lds + (w * 4 + g) * 1024 + (16 * q + c) * 16);
#pragma unroll
      for (int sw = 1; sw < 4; ++sw) r[q] += *(const f32x4*)(lds + sw * RS + (w * 4 + g) * 1024 + (16 * q + c) * 16);
    }
    u32x4 o[2];
#pragma unroll
    for (int t = 0; t < 8; ++t) {
      const u32 bb = t < 4 ? bv0[t & 3] : bv1[t & 3];
      const u32 pr = pk2(r[t >> 1][(2 * t) & 3], r[t >> 1][(2 * t + 1) & 3]);
      const u32 ps = pk2(blo(pr) + blo(bb), bhi(pr) + bhi(bb));
      const float u0 = blo(ps), u1 = bhi(ps);
      const float k0 = -2.302208198f, k1c = -0.1029432397f;
      const float e0 = __builtin_amdgcn_exp2f(u0 * (k0 + k1c * u0 * u0));
      const float e1 = __builtin_amdgcn_exp2f(u1 * (k0 + k1c * u1 * u1));
      o[t >> 2][t & 3] = pk2(u0 * __builtin_amdgcn_rcpf(1.f + e0), u1 * __builtin_amdgcn_rcpf(1.f + e1));
    }
    char* up = ws + WS_U + ((size_t)(F0 / 8 + 2 * g) * 512 + D0 + 16 * w + c) * 16;
    *(u32x4*)up = o[0];
    *(u32x4*)(up + 8192) = o[1];
    STAMP(1, 5);
  } else {
    const int h = xcd * 4 + (slot - 28);
    const rsrc_t rk = mkrs(ws + WS_KVP + (size_t)h * 65536, 65536), rq = mkrs(ws + WS_PHIQ + (size_t)h * 131072, 131072);
    u32x4 kp[16];
#pragma unroll
    for (int n = 0; n < 16; ++n) kp[n] = ld16(rk, (tid >> 7) * 32768 + (tid & 127) * 16, n * 2048);
    const f32x4 zp = *(const f32x4*)(ws + WS_ZP + (size_t)h * 4096 + tid * 16);
    u32x4 ph[32];
#pragma unroll
    for (int n = 0; n < 32; ++n) ph[n] = ld16(rq, c * 64 + g * 16, (w * 32 + n) * 1024);
    f32x4 s = __builtin_bit_cast(f32x4, kp[0]);
#pragma unroll
    for (int n = 1; n < 16; ++n) s += __builtin_bit_cast(f32x4, kp[n]);
    *(f32x4*)(lds + tid * 16) = s;
    *(f32x4*)(lds + 4096 + tid * 16) = zp;
    RAW_BARRIER();
    if (tid < 32) {
      float t = 0.f;
#pragma unroll
      for (int n = 0; n < 32; ++n) t += *(const float*)(lds + 4096 + n * 128 + tid * 4);
      *(float*)(lds + 8192 + tid * 4) = t;
    }
    RAW_BARRIER();
    STAMP(1, 6);
    u32x4 akv, az;
    {
      const int o0 = ((g >> 1) * 256 + 2 * (g & 1) * 64 + c * 4) * 4;
      const f32x4 k0 = *(const f32x4*)(lds + o0) + *(const f32x4*)(lds + 2048 + o0);
      const f32x4 k1v = *(const f32x4*)(lds + o0 + 256) + *(const f32x4*)(lds + 2048 + o0 + 256);
      akv = u32x4{pk2(k0[0], k0[1]), pk2(k0[2], k0[3]), pk2(k1v[0], k1v[1]), pk2(k1v[2], k1v[3])};
      const f32x4 z0 = *(const f32x4*)(lds + 8192 + g * 32), z1 = *(const f32x4*)(lds + 8192 + g * 32 + 16);
      az = u32x4{pk2(z0[0], z0[1]), pk2(z0[2], z0[3]), pk2(z1[0], z1[1]), pk2(z1[2], z1[3])};
    }
    char* op = ws + WS_O + ((size_t)(2 * h + (g >> 1)) * 2048 + w * 512 + c) * 16 + (g & 1) * 8;
#pragma unroll
    for (int n = 0; n < 32; ++n) {
      const f32x4 num = mfma(akv, ph[n], f32x4{0.f, 0.f, 0.f, 0.f});
      const f32x4 den = mfma(az, ph[n], f32x4{0.f, 0.f, 0.f, 0.f});
      const float rd = 1.f / rbf(rbf(den[0]) + 1e-6f);
      const u32 n0 = pk2(num[0], num[1]), n1 = pk2(num[2], num[3]);
      *(u32x2*)(op + n * 256) = u32x2{pk2(blo(n0) * rd, bhi(n0) * rd), pk2(blo(n1) * rd, bhi(n1) * rd)};
    }
    STAMP(1, 7);
  }
  STAMP_OUT(1);
}

__global__ __launch_bounds__(256, 1) void k3(const u16* __restrict__ w2, const u16* __restrict__ wo, const u16* __restrict__ b2,
                                              const char* __restrict__ ws, u16* __restrict__ out) {
  __shared__ __attribute__((aligned(16))) char lds[4 * RS];
  const int tid = threadIdx.x, lane = tid & 63, c = lane & 15, g = lane >> 4;
  const int w = __builtin_amdgcn_readfirstlane(tid >> 6);
  const int b = blockIdx.x, xcd = b & 7, slot = b >> 3;
  const int S0 = (xcd * 4 + (slot & 3)) * 64, D0 = (slot >> 2) * 64;
  const rsrc_t rw2 = mkrs(w2, F_ * S_ * 2), ru = mkrs(ws + WS_U, 1835008);
  const rsrc_t rwo = mkrs(wo, D_ * D_ * 2), ro = mkrs(ws + WS_O, 2097152);
  const int v2e = dma_voff(lane, S_ * 2, 0) + S0 * 2, v2o = dma_voff(lane, S_ * 2, 1) + S0 * 2, vu = (g * 512 + D0 + c) * 16;
  const int voe = dma_voff(lane, D_ * 2, 0) + D0 * 2, voo = dma_voff(lane, D_ * 2, 1) + D0 * 2, vo = (g * 2048 + S0 + c) * 16;
  constexpr int NY = 14, NA = 4, NS = NY + NA;
  const int sw2 = w * NY * 32 * S_ * 2, su = w * NY * 32768, swo = w * NA * 32 * D_ * 2, so = w * NA * 131072;
  STAMP_DECL;
  STAMP(2, 0);
  char* ring = lds + w * RS;
  u32x4 pb[PD][4];
  auto issue = [&](int s) {
    if (s < NY) {
      dma_stage(ring + (s % PD) * 4096, rw2, v2e, v2o, sw2 + s * 32 * S_ * 2, S_ * 2);
      load_p(pb[s % PD], ru, vu, su + s * 32768);
    } else {
      dma_stage(ring + (s % PD) * 4096, rwo, voe, voo, swo + (s - NY) * 32 * D_ * 2, D_ * 2);
      load_p(pb[s % PD], ro, vo, so + (s - NY) * 131072);
    }
  };
#pragma unroll
  for (int s = 0; s < PD; ++s) { issue(s); SCHED_FENCE(); }
  f32x4 ay[4][4], aa[4][4];
#pragma unroll
  for (int j = 0; j < 4; ++j)
#pragma unroll
    for (int i = 0; i < 4; ++i) { ay[j][i] = f32x4{0.f, 0.f, 0.f, 0.f}; aa[j][i] = f32x4{0.f, 0.f, 0.f, 0.f}; }
  u32 ta[4];
  tr_addr(ta, ldsaddr(ring), lane);
  STAMP(2, 1);
  float gv[4], gown, bown;
  static_for<0, NS>([&](auto sc) {
    constexpr int s = decltype(sc)::value;
    if (s == 1) STAMP(2, 2);
    if (s == 9) STAMP(2, 3);
    u32x4 wf[4], pf[4];
    TrFrag tf;
    wait_vm<4 + 8 * imin(PD - 1, NS - 1 - s)>();
    tr_issue<(s % PD) * 4096>(tf, ta);
    tr_wait(tf, wf);
#pragma unroll
    for (int i = 0; i < 4; ++i) pf[i] = pb[s % PD][i];
    if constexpr (s + PD < NS) issue(s + PD);
    if constexpr (s == NS - 1) {
#pragma unroll
      for (int j = 0; j < 4; ++j) gv[j] = *(const float*)(ws + WS_G + (S0 + 16 * j + c) * 4);
      gown = *(const float*)(ws + WS_G + (S0 + 16 * w + c) * 4);
      bown = blo(b2[S0 + 16 * w + c]);
    }
    SCHED_FENCE();
#pragma unroll
    for (int j = 0; j < 4; ++j)
#pragma unroll
      for (int i = 0; i < 4; ++i) {
        if (s < NY) ay[i][j] = mfma(pf[i], wf[j], ay[i][j]);
        else aa[j][i] = mfma(wf[j], pf[i], aa[j][i]);
      }
    SCHED_FENCE();
  });
  STAMP(2, 4);
#pragma unroll
  for (int j = 0; j < 4; ++j) {
    const float ga = gv[j], gb = rbf(1.f - ga);
#pragma unroll
    for (int i = 0; i < 4; ++i) *(f32x4*)(ring + (j * 4 + i) * 1024 + lane * 16) = ga * aa[i][j] + gb * ay[i][j];
  }
  RAW_BARRIER();
  f32x4 r[4];
  const float t = rbf(1.f - gown) * bown;
#pragma unroll
  for (int q = 0; q < 4; ++q) {
    r[q] = *(const f32x4*)(lds + (w * 4 + g) * 1024 + (16 * q + c) * 16) + t;
#pragma unroll
    for (int sw = 1; sw < 4; ++sw) r[q] += *(const f32x4*)(lds + sw * RS + (w * 4 + g) * 1024 + (16 * q + c) * 16);
  }
  u16* op = out + (size_t)(S0 + 16 * w + c) * 512 + D0 + 16 * g;
  *(u32x4*)op = u32x4{pk2(r[0][0], r[0][1]), pk2(r[0][2], r[0][3]), pk2(r[1][0], r[1][1]), pk2(r[1][2], r[1][3])};
  *(u32x4*)(op + 8) = u32x4{pk2(r[2][0], r[2][1]), pk2(r[2][2], r[2][3]), pk2(r[3][0], r[3][1]), pk2(r[3][2], r[3][3])};
  STAMP(2, 5);
  STAMP_OUT(2);
}

extern "C" void kernel_launch(void* const* d_in, const int* in_sizes, int n_in, void* d_out, int out_size, void* d_ws,
                              size_t ws_size, hipStream_t stream) {
  const u16* x = (const u16*)d_in[0];
  const u16* wqkv = (const u16*)d_in[1];
  const u16* wphi = (const u16*)d_in[2];
  const u16* wo = (const u16*)d_in[3];
  const u16* w1 = (const u16*)d_in[5];
  const u16* b1 = (const u16*)d_in[6];
  const u16* w2 = (const u16*)d_in[7];
  const u16* b2 = (const u16*)d_in[8];
  const u16* gw = (const u16*)d_in[9];
  char* ws = (char*)d_ws;
  k1<<<256, 512, 0, stream>>>(x, wqkv, wphi, gw, ws);
  k2<<<256, 256, 0, stream>>>(w1, b1, ws);
  k3<<<256, 256, 0, stream>>>(w2, wo, b2, ws, (u16*)d_out);
}
```

```cpp
#include <hip/hip_runtime.h>
#include <hip/hip_bf16.h>
#include <type_traits>

typedef unsigned short u16;
typedef unsigned int u32;
typedef __attribute__((ext_vector_type(8))) __bf16 bf16x8;
typedef __attribute__((ext_vector_type(2))) __bf16 bf16x2;
typedef __attribute__((ext_vector_type(4))) short s16x4;
typedef __attribute__((ext_vector_type(4))) float f32x4;
typedef __attribute__((ext_vector_type(2))) float f32x2;
typedef __attribute__((ext_vector_type(4))) unsigned int u32x4;
typedef __attribute__((ext_vector_type(2))) unsigned int u32x2;
typedef __amdgpu_buffer_rsrc_t rsrc_t;

#define DEVI __device__ __forceinline__
#define SCHED_FENCE() __builtin_amdgcn_sched_barrier(0)
#define RAW_BARRIER() do { asm volatile("s_waitcnt lgkmcnt(0)" ::: "memory"); __builtin_amdgcn_s_barrier(); asm volatile("" ::: "memory"); } while (0)

#define STAMP(k, s)
#define STAMP_DECL
#define STAMP_OUT(k)
constexpr int S_ = 2048, D_ = 512, F_ = 1792;
constexpr size_t WS_H = 0;
constexpr size_t WS_PHIQ = WS_H + 2097152;
constexpr size_t WS_KVP = WS_PHIQ + 4194304;
constexpr size_t WS_ZP = WS_KVP + 2097152;
constexpr size_t WS_G = WS_ZP + 131072;
constexpr size_t WS_U = WS_G + 8192;
constexpr size_t WS_O = WS_U + 1835008;

DEVI rsrc_t mkrs(const void* p, int bytes) { return __builtin_amdgcn_make_buffer_rsrc((void*)p, (short)0, bytes, 0x00020000); }
DEVI u32x4 ld16(rsrc_t r, int v, int s) { return __builtin_amdgcn_raw_buffer_load_b128(r, v, s, 0); }
DEVI u32x2 ld8(rsrc_t r, int v, int s) { return __builtin_amdgcn_raw_buffer_load_b64(r, v, s, 0); }
DEVI u32 pk2(float lo, float hi) { f32x2 v = {lo, hi}; return __builtin_bit_cast(u32, __builtin_convertvector(v, bf16x2)); }
DEVI float blo(u32 v) { return __builtin_bit_cast(float, v << 16); }
DEVI float bhi(u32 v) { return __builtin_bit_cast(float, v & 0xffff0000u); }
DEVI float rbf(float x) { return blo(pk2(x, x)); }
DEVI f32x4 mfma(u32x4 a, u32x4 b, f32x4 c) {
  return __builtin_amdgcn_mfma_f32_16x16x32_bf16(__builtin_bit_cast(bf16x8, a), __builtin_bit_cast(bf16x8, b), c, 0, 0, 0);
}
DEVI void wperm(const u32x2* w, u32x4* f) {
#pragma unroll
  for (int t = 0; t < 4; ++t) {
    f[0][t] = __builtin_amdgcn_perm(w[2 * t + 1].x, w[2 * t].x, 0x05040100u);
    f[1][t] = __builtin_amdgcn_perm(w[2 * t + 1].x, w[2 * t].x, 0x07060302u);
    f[2][t] = __builtin_amdgcn_perm(w[2 * t + 1].y, w[2 * t].y, 0x05040100u);
    f[3][t] = __builtin_amdgcn_perm(w[2 * t + 1].y, w[2 * t].y, 0x07060302u);
  }
}
template <int CTRL> DEVI float dppf(float x) {
  return __builtin_bit_cast(float, __builtin_amdgcn_update_dpp(0, __builtin_bit_cast(int, x), CTRL, 0xf, 0xf, true));
}
DEVI float wave_sum(float x) {
  x += dppf<0xB1>(x); x += dppf<0x4E>(x); x += dppf<0x141>(x); x += dppf<0x140>(x);
  u32 b = __builtin_bit_cast(u32, x);
  auto r = __builtin_amdgcn_permlane16_swap(b, b, false, false);
  x = __builtin_bit_cast(float, (u32)r[0]) + __builtin_bit_cast(float, (u32)r[1]);
  b = __builtin_bit_cast(u32, x);
  auto q = __builtin_amdgcn_permlane32_swap(b, b, false, false);
  return __builtin_bit_cast(float, (u32)q[0]) + __builtin_bit_cast(float, (u32)q[1]);
}
typedef __attribute__((address_space(3))) char lchar;
DEVI u32 ldsaddr(const char* p) { return (u32)(size_t)(const lchar*)p; }
template <int N> DEVI void wait_vm() { asm volatile("s_waitcnt vmcnt(%0)" ::"n"(N) : "memory"); }
DEVI void dma_stage(char* dst, rsrc_t r, int ve, int vo, int so, int ld) {
#pragma unroll
  for (int n = 0; n < 4; ++n)
    __builtin_amdgcn_raw_ptr_buffer_load_lds(r, (lchar*)(dst + n * 1024), 16, (n & 1) ? vo : ve, so + n * 8 * ld, 0, 0);
}
DEVI int dma_voff(int lane, int ld, int odd) { return (lane >> 3) * ld + (((lane & 7) ^ ((((lane >> 4) & 1) | (odd << 1)) << 1)) << 4); }
DEVI void tr_addr(u32* ta, u32 base, int lane) {
  const int kg = lane >> 4, q = (lane >> 2) & 3, pp = lane & 3;
  const int fr = ((q >> 1) & 1) | ((kg & 1) << 1);
#pragma unroll
  for (int j = 0; j < 4; ++j) ta[j] = base + (8 * kg + q) * 128 + (((2 * j + (pp >> 1)) ^ (fr << 1)) << 4) + (pp & 1) * 8;
}
struct TrFrag { u32x2 h[8]; };
template <int OFF> DEVI void tr_issue(TrFrag& t, const u32* ta) {
  asm volatile(
      "ds_read_b64_tr_b16 %0, %8 offset:%12\n\tds_read_b64_tr_b16 %1, %8 offset:%13\n\t"
      "ds_read_b64_tr_b16 %2, %9 offset:%12\n\tds_read_b64_tr_b16 %3, %9 offset:%13\n\t"
      "ds_read_b64_tr_b16 %4, %10 offset:%12\n\tds_read_b64_tr_b16 %5, %10 offset:%13\n\t"
      "ds_read_b64_tr_b16 %6, %11 offset:%12\n\tds_read_b64_tr_b16 %7, %11 offset:%13"
      : "=&v"(t.h[0]), "=&v"(t.h[1]), "=&v"(t.h[2]), "=&v"(t.h[3]), "=&v"(t.h[4]), "=&v"(t.h[5]), "=&v"(t.h[6]), "=&v"(t.h[7])
      : "v"(ta[0]), "v"(ta[1]), "v"(ta[2]), "v"(ta[3]), "n"(OFF), "n"(OFF + 512)
      : "memory");
}
DEVI void tr_wait(TrFrag& t, u32x4* f) {
  asm volatile("s_waitcnt lgkmcnt(0)"
               : "+v"(t.h[0]), "+v"(t.h[1]), "+v"(t.h[2]), "+v"(t.h[3]), "+v"(t.h[4]), "+v"(t.h[5]), "+v"(t.h[6]), "+v"(t.h[7])
               :: "memory");
#pragma unroll
  for (int j = 0; j < 4; ++j) f[j] = u32x4{t.h[2 * j].x, t.h[2 * j].y, t.h[2 * j + 1].x, t.h[2 * j + 1].y};
}
template <int OFF> DEVI void x_issue(u32x4* xf, u32 a) {
  asm volatile("ds_read_b128 %0, %4 offset:%5\n\tds_read_b128 %1, %4 offset:%6\n\tds_read_b128 %2, %4 offset:%7\n\tds_read_b128 %3, %4 offset:%8"
               : "=&v"(xf[0]), "=&v"(xf[1]), "=&v"(xf[2]), "=&v"(xf[3])
               : "v"(a), "n"(OFF), "n"(OFF + 4096), "n"(OFF + 8192), "n"(OFF + 12288) : "memory");
}
template <int OFF> DEVI void tr1(u32x2& d, u32 a) { asm volatile("ds_read_b64_tr_b16 %0, %1 offset:%2" : "=v"(d) : "v"(a), "n"(OFF) : "memory"); }
template <int OFF> DEVI void x1(u32x4& d, u32 a) { asm volatile("ds_read_b128 %0, %1 offset:%2" : "=v"(d) : "v"(a), "n"(OFF) : "memory"); }
DEVI void dma1(char* dst, rsrc_t r, int v, int so) { __builtin_amdgcn_raw_ptr_buffer_load_lds(r, (lchar*)dst, 16, v, so, 0, 0); }
DEVI u32 touch(const void* base, int bytes, int off) { return __builtin_amdgcn_raw_buffer_load_b32(mkrs(base, bytes), off, 0, 0); }
DEVI void keep(u32 v) { asm volatile("" ::"v"(v)); }
template <int I, int N, class Fn> DEVI void static_for(Fn&& f) {
  if constexpr (I < N) { f(std::integral_constant<int, I>{}); static_for<I + 1, N>(f); }
}
constexpr int imin(int a, int b) { return a < b ? a : b; }

constexpr int HS = 1152;
constexpr int L1_X = 0;
constexpr int PD1 = 7;
constexpr int CODE_BYTES = 42880;
constexpr int L1_R = 65536;
constexpr int RS1 = PD1 * 4096;
constexpr int L1_PH = L1_X;
constexpr int L1_WF = L1_R + 3 * RS1;
constexpr int L1_SIZE = L1_WF + 8192;

__global__ __launch_bounds__(512, 1) void k1(const u16* __restrict__ x, const u16* __restrict__ wqkv,
                                              const u16* __restrict__ wphi, const u16* __restrict__ gw,
                                              const u16* __restrict__ w1, const u16* __restrict__ b1,
                                              const u16* __restrict__ w2, const u16* __restrict__ b2,
                                              const u16* __restrict__ wo, char* __restrict__ ws) {
  __shared__ __attribute__((aligned(16))) char lds[L1_SIZE];
  const int tid = threadIdx.x, lane = tid & 63, c = lane & 15, g = lane >> 4;
  const int w = __builtin_amdgcn_readfirstlane(tid >> 6);
  const int b = blockIdx.x, xcd = b & 7, slot = b >> 3;
  const int rb = (xcd >> 1) * 8 + (slot & 7), hg = (xcd & 1) * 4 + (slot >> 3);

  STAMP_DECL;
  STAMP(0, 0);
  u32 pf[7] = {0u, 0u, 0u, 0u, 0u, 0u, 0u};
  if (w < 3) {
    const rsrc_t rw = mkrs(wqkv, 512 * 1536 * 2);
    const int cb = (w * 512 + hg * 64) * 2;
    const int ve = dma_voff(lane, 3072, 0) + cb, vo = dma_voff(lane, 3072, 1) + cb;
    char* ring = lds + L1_R + w * RS1;
#pragma unroll
    for (int s = 0; s < PD1; ++s) dma_stage(ring + s * 4096, rw, ve, vo, s * 32 * 3072, 3072);
    f32x4 acc[4][4];
#pragma unroll
    for (int j = 0; j < 4; ++j)
#pragma unroll
      for (int t = 0; t < 4; ++t) acc[j][t] = f32x4{0.f, 0.f, 0.f, 0.f};
    u32 xa[4], ta[4];
#pragma unroll
    for (int t = 0; t < 4; ++t) xa[t] = ldsaddr(lds) + c * 256 + (((4 * t + g) ^ c) << 4);
    tr_addr(ta, ldsaddr(ring), lane);
    STAMP(0, 1);
    RAW_BARRIER();
    STAMP(0, 2);
    TrFrag tf[2];
    u32x4 xf[2][4];
    wait_vm<4 * (PD1 - 1)>();
    tr_issue<0>(tf[0], ta);
    x_issue<0>(xf[0], xa[0]);
    static_for<0, 16>([&](auto sc) {
      constexpr int s = decltype(sc)::value;
      u32x4 wf[4];
      tr_wait(tf[s & 1], wf);
      asm volatile("" : "+v"(xf[s & 1][0]), "+v"(xf[s & 1][1]), "+v"(xf[s & 1][2]), "+v"(xf[s & 1][3]));
      static_for<0, 16>([&](auto mc) {
        constexpr int m = decltype(mc)::value;
        acc[m >> 2][m & 3] = mfma(wf[m >> 2], xf[s & 1][m & 3], acc[m >> 2][m & 3]);
        if constexpr (s + 1 < 16) {
          if constexpr (m == 0 && s + 1 == 4) { __builtin_amdgcn_s_barrier(); STAMP(0, 3); }
          if constexpr (m < 4) x1<((s + 1) >> 2) * 16384 + m * 4096>(xf[(s + 1) & 1][m], xa[(s + 1) & 3]);
          if constexpr (m == 3) wait_vm<4 * imin(PD1 - 2, 14 - s)>();
          if constexpr (m >= 4 && m < 8) {
            tr1<((s + 1) % PD1) * 4096>(tf[(s + 1) & 1].h[2 * (m - 4)], ta[m - 4]);
            tr1<((s + 1) % PD1) * 4096 + 512>(tf[(s + 1) & 1].h[2 * (m - 4) + 1], ta[m - 4]);
          }
        }
        if constexpr (s + PD1 < 16 && m >= 8 && m < 12)
          dma1(ring + (s % PD1) * 4096 + (m - 8) * 1024, rw, (m & 1) ? vo : ve, ((s + PD1) * 32 + (m - 8) * 8) * 3072);
        SCHED_FENCE();
      });
    });
#pragma unroll
    for (int t = 0; t < 4; ++t)
#pragma unroll
      for (int j = 0; j < 4; ++j)
        *(u32x2*)(ring + (t * 2 + (g >> 1)) * HS + (16 * j + c) * 16 + (g & 1) * 8) =
            u32x2{pk2(acc[j][t][0], acc[j][t][1]), pk2(acc[j][t][2], acc[j][t][3])};
  } else {
    u32x4 gv;
    u32x2 wp[16];
    if ((w & 3) == 3) gv = *(const u32x4*)(gw + lane * 8);
    if (w == 4) {
      const rsrc_t rp = mkrs(wphi, 32 * 256 * 2);
      const int voff = ((4 * hg + g) * 256 + 4 * (c & 3)) * 2;
#pragma unroll
      for (int d = 0; d < 16; ++d) wp[d] = ld8(rp, voff + d * 32, 0);
    }
    SCHED_FENCE();
    if (w >= 4) {
#pragma unroll
      for (int kq = 0; kq < 4; ++kq)
#pragma unroll
        for (int i = 0; i < 4; ++i) {
          const int pg = (w - 4) * 4 + i, p = 4 * pg + g;
          const int row = rb * 64 + p;
          const u16* src = x + row * 512 + (16 * kq + (c ^ (p & 15))) * 8;
          __builtin_amdgcn_global_load_lds((const u32*)src, (__attribute__((address_space(3))) u32*)(lds + L1_X + kq * 16384 + pg * 1024), 16, 0, 0);
        }
      asm volatile("s_waitcnt vmcnt(12)" ::: "memory");
      RAW_BARRIER();
      asm volatile("s_waitcnt vmcnt(0)" ::: "memory");
      RAW_BARRIER();
      if (w == 5) {
#pragma unroll
        for (int i = 0; i < 7; ++i) {
          const int idx = i * 64 + lane, r = idx / 7;
          pf[i] = touch(w1, S_ * F_ * 2, (slot * 64 + r) * (F_ * 2) + (xcd >> 1) * 896 + (idx - r * 7) * 128);
        }
      } else if (w == 6) {
#pragma unroll
        for (int i = 0; i < 4; ++i) {
          const int idx = i * 64 + lane;
          pf[i] = touch(w2, S_ * F_ * 2, idx < 224 ? (slot * 56 + (idx >> 2)) * (S_ * 2) + xcd * 512 + (idx & 3) * 128 : 0x7fffffff);
        }
        pf[4] = touch(wo, D_ * D_ * 2, lane < 16 ? (b * 16 + lane) * 128 : 0x7fffffff);
        pf[5] = touch(b1, F_ * 2, lane * 128);
        pf[6] = touch(b2, S_ * 2, lane * 128);
      }
    } else {
#pragma unroll
      for (int i = 0; i < 2; ++i) {
        const int idx = i * 64 + lane, r = idx / 3;
        pf[i] = touch(wqkv, 512 * 1536 * 2, idx < 108 ? (224 + (slot & 7) * 36 + r) * 3072 + (idx - r * 3) * 1024 + hg * 128 : 0x7fffffff);
      }
      pf[2] = touch((const void*)(__builtin_amdgcn_s_getpc() & ~127ull), CODE_BYTES, lane < 12 ? (slot * 12 + lane) * 128 : 0x7fffffff);
      RAW_BARRIER();
      RAW_BARRIER();
    }
    if (w == 4) {
      const bool on = (g == (c >> 2));
#pragma unroll
      for (int h = 0; h < 2; ++h) {
        u32x4 f[4];
        wperm(wp + 8 * h, f);
#pragma unroll
        for (int jj = 0; jj < 4; ++jj) {
          u32x4 v = on ? f[jj] : u32x4{0u, 0u, 0u, 0u};
          *(u32x4*)(lds + L1_WF + (jj * 2 + h) * 1024 + lane * 16) = v;
        }
      }
    } else if ((w & 3) == 3) {
      const int hf = w >> 2;
      float xv[4][8], sum[4], gs[4];
#pragma unroll
      for (int q = 0; q < 4; ++q) {
        const int p = 8 * hg + 4 * hf + q;
        const u32x4 v = *(const u32x4*)(lds + L1_X + g * 16384 + p * 256 + ((c ^ (p & 15)) << 4));
        float s = 0.f, t = 0.f;
#pragma unroll
        for (int e = 0; e < 4; ++e) {
          xv[q][2 * e] = blo(v[e]); xv[q][2 * e + 1] = bhi(v[e]);
          s += xv[q][2 * e] + xv[q][2 * e + 1];
          const u32 pr = pk2(xv[q][2 * e] * blo(gv[e]), xv[q][2 * e + 1] * bhi(gv[e]));
          t += blo(pr) + bhi(pr);
        }
        sum[q] = s; gs[q] = t;
      }
      float var[4];
#pragma unroll
      for (int q = 0; q < 4; ++q) { sum[q] = wave_sum(sum[q]) * (1.f / 512.f); gs[q] = wave_sum(gs[q]); }
#pragma unroll
      for (int q = 0; q < 4; ++q) {
        float s = 0.f;
#pragma unroll
        for (int e = 0; e < 8; ++e) { const float d = xv[q][e] - sum[q]; s += d * d; }
        var[q] = wave_sum(s) * (1.f / 512.f);
      }
#pragma unroll
      for (int q = 0; q < 4; ++q) {
        const float m = rbf(sum[q]);
        const float rs = 1.f / rbf(sqrtf(rbf(rbf(var[q]) + 1e-5f)));
#pragma unroll
        for (int e = 0; e < 4; ++e) {
          const u32 pr = pk2(xv[q][2 * e] - m, xv[q][2 * e + 1] - m);
          xv[q][2 * e] = blo(pr) * rs; xv[q][2 * e + 1] = bhi(pr) * rs;
        }
      }
      char* hp = ws + WS_H + (size_t)(rb * 8 + hg) * 8192 + lane * 128 + hf * 8;
#pragma unroll
      for (int e = 0; e < 8; ++e)
        *(u32x2*)(hp + e * 16) = u32x2{pk2(xv[0][e], xv[1][e]), pk2(xv[2][e], xv[3][e])};
      if (lane == 0) {
        f32x4 o;
#pragma unroll
        for (int q = 0; q < 4; ++q) o[q] = rbf(1.f / (1.f + __expf(-rbf(gs[q]))));
        *(f32x4*)(ws + WS_G + (rb * 64 + 8 * hg + 4 * hf) * 4) = o;
      }
    }
  }
  STAMP(0, 4);
  RAW_BARRIER();
  STAMP(0, 5);
  {
    const int mat = w >> 2, tau = w & 3;
    const u32x4 b0 = *(const u32x4*)(lds + L1_R + mat * RS1 + (tau * 2) * HS + lane * 16);
    const u32x4 b1 = *(const u32x4*)(lds + L1_R + mat * RS1 + (tau * 2 + 1) * HS + lane * 16);
    float p[16];
#pragma unroll
    for (int jj = 0; jj < 4; ++jj) {
      const u32x4 a0 = *(const u32x4*)(lds + L1_WF + (jj * 2) * 1024 + lane * 16);
      const u32x4 a1 = *(const u32x4*)(lds + L1_WF + (jj * 2 + 1) * 1024 + lane * 16);
      f32x4 r = mfma(a1, b1, mfma(a0, b0, f32x4{0.f, 0.f, 0.f, 0.f}));
      const u32 u0 = pk2(r[0], r[1]), u1 = pk2(r[2], r[3]);
      p[jj] = blo(u0); p[4 + jj] = bhi(u0); p[8 + jj] = blo(u1); p[12 + jj] = bhi(u1);
    }
    float mx = p[0], mn = p[0];
#pragma unroll
    for (int f = 1; f < 16; ++f) { mx = fmaxf(mx, p[f]); mn = fminf(mn, p[f]); }
    const float L2E = 1.4426950408889634f;
    float ep[16], en[16], sp = 0.f, sn = 0.f;
#pragma unroll
    for (int f = 0; f < 16; ++f) {
      ep[f] = __builtin_amdgcn_exp2f((p[f] - mx) * L2E);
      en[f] = __builtin_amdgcn_exp2f((mn - p[f]) * L2E);
      sp += ep[f]; sn += en[f];
    }
    const float rp = __builtin_amdgcn_rcpf(sp), rn = __builtin_amdgcn_rcpf(sn);
    u32x4 ph[4];
#pragma unroll
    for (int t = 0; t < 4; ++t) {
      ph[0][t] = pk2(ep[2 * t] * rp, ep[2 * t + 1] * rp);
      ph[1][t] = pk2(ep[8 + 2 * t] * rp, ep[9 + 2 * t] * rp);
      ph[2][t] = pk2(en[2 * t] * rn, en[2 * t + 1] * rn);
      ph[3][t] = pk2(en[8 + 2 * t] * rn, en[9 + 2 * t] * rn);
    }
    if (mat == 0) {
      char* dst = ws + WS_PHIQ + ((size_t)(4 * hg + g) * 2048 + rb * 64 + 16 * tau + c) * 64;
#pragma unroll
      for (int pc = 0; pc < 4; ++pc) *(u32x4*)(dst + pc * 16) = ph[pc];
    } else {
#pragma unroll
      for (int pc = 0; pc < 4; ++pc) *(u32x4*)(lds + L1_PH + (tau * 4 + pc) * HS + lane * 16) = ph[pc];
    }
  }
  STAMP(0, 6);
  RAW_BARRIER();
  {
    const int hh = w & 3, sign = w >> 2;
    const int q = (lane >> 2) & 3, pp = lane & 3;
    const int ro = (pp >> 1) * HS + (16 * hh + 4 * g + q) * 16 + (pp & 1) * 8;
    f32x4 kv = {0.f, 0.f, 0.f, 0.f}, z = {0.f, 0.f, 0.f, 0.f};
    const u32x4 ones = {0x3f803f80u, 0x3f803f80u, 0x3f803f80u, 0x3f803f80u};
#pragma unroll
    for (int sg = 0; sg < 2; ++sg) {
      u32x2 a0, a1, v0, v1;
      const u32 pa = ldsaddr(lds + L1_PH) + (8 * sg + 2 * sign) * HS + ro, pv = ldsaddr(lds + L1_R + 2 * RS1) + 4 * sg * HS + ro;
      asm volatile("ds_read_b64_tr_b16 %0, %4\n\tds_read_b64_tr_b16 %1, %4 offset:%6\n\tds_read_b64_tr_b16 %2, %5\n\t"
                   "ds_read_b64_tr_b16 %3, %5 offset:%7\n\ts_waitcnt lgkmcnt(0)"
                   : "=&v"(a0), "=&v"(a1), "=&v"(v0), "=&v"(v1) : "v"(pa), "v"(pv), "n"(4 * HS), "n"(2 * HS) : "memory");
      const u32x4 a = {a0.x, a0.y, a1.x, a1.y}, v = {v0.x, v0.y, v1.x, v1.y};
      kv = mfma(a, v, kv);
      z = mfma(a, ones, z);
    }
    const int hb = (4 * hg + hh) * 32 + rb;
    *(f32x4*)(ws + WS_KVP + ((size_t)hb * 512 + sign * 256 + lane * 4) * 4) = kv;
    if (c == 0) *(f32x4*)(ws + WS_ZP + ((size_t)hb * 32 + sign * 16 + 4 * g) * 4) = z;
  }
#pragma unroll
  for (int i = 0; i < 7; ++i) keep(pf[i]);
  STAMP(0, 7);
  STAMP_OUT(0);
}

constexpr int PD = 6;
constexpr int RS = PD * 4096;
DEVI void load_p(u32x4* p, rsrc_t rp, int vp, int sp) {
#pragma unroll
  for (int i = 0; i < 4; ++i) p[i] = ld16(rp, vp + i * 256, sp);
}

__global__ __launch_bounds__(256, 1) void k2(const u16* __restrict__ w1, const u16* __restrict__ b1, const u16* __restrict__ w2,
                                              const u16* __restrict__ wo, char* __restrict__ ws) {
  __shared__ __attribute__((aligned(16))) char lds[4 * RS];
  const int tid = threadIdx.x, lane = tid & 63, c = lane & 15, g = lane >> 4;
  const int w = __builtin_amdgcn_readfirstlane(tid >> 6);
  const int b = blockIdx.x, xcd = b & 7, slot = b >> 3;
  STAMP_DECL;
  STAMP(1, 0);
  const u32 pfh = touch(ws + WS_H, 2097152, (slot * 8 + (tid >> 5)) * 8192 + (xcd & 1) * 4096 + (tid & 31) * 128);
  if (slot < 28) {
    const int D0 = ((xcd & 1) * 4 + (slot & 3)) * 64, F0 = ((xcd >> 1) * 7 + (slot >> 2)) * 64;
    const rsrc_t rw = mkrs(w1, S_ * F_ * 2), rp = mkrs(ws + WS_H, 2097152);
    const int ve = dma_voff(lane, F_ * 2, 0) + F0 * 2, vo = dma_voff(lane, F_ * 2, 1) + F0 * 2, vp = (g * 512 + D0 + c) * 16;
    constexpr int NS = 16;
    const int sw0 = w * NS * 32 * F_ * 2, sp0 = w * NS * 32768;
    const u32x4 bv0 = *(const u32x4*)(b1 + F0 + 16 * g), bv1 = *(const u32x4*)(b1 + F0 + 16 * g + 8);
    SCHED_FENCE();
    char* ring = lds + w * RS;
    u32x4 pb[PD][4];
#pragma unroll
    for (int s = 0; s < PD; ++s) {
      dma_stage(ring + s * 4096, rw, ve, vo, sw0 + s * 32 * F_ * 2, F_ * 2);
      load_p(pb[s], rp, vp, sp0 + s * 32768);
      SCHED_FENCE();
    }
    f32x4 acc[4][4];
#pragma unroll
    for (int j = 0; j < 4; ++j)
#pragma unroll
      for (int i = 0; i < 4; ++i) acc[j][i] = f32x4{0.f, 0.f, 0.f, 0.f};
    u32 ta[4];
    tr_addr(ta, ldsaddr(ring), lane);
    STAMP(1, 1);
    auto issue1 = [&](int s, int m) {
      if (m < 4) dma1(ring + (s % PD) * 4096 + m * 1024, rw, (m & 1) ? vo : ve, sw0 + (s * 32 + m * 8) * F_ * 2);
      else pb[s % PD][m - 4] = ld16(rp, vp + (m - 4) * 256, sp0 + s * 32768);
    };
    TrFrag tf[2];
    wait_vm<4 + 8 * (PD - 1)>();
    tr_issue<0>(tf[0], ta);
    static_for<0, NS>([&](auto sc) {
      constexpr int s = decltype(sc)::value;
      if (s == 1) STAMP(1, 2);
      if (s == 8) STAMP(1, 3);
      u32x4 wf[4], pf[4];
      tr_wait(tf[s & 1], wf);
#pragma unroll
      for (int i = 0; i < 4; ++i) pf[i] = pb[s % PD][i];
      static_for<0, 16>([&](auto mc) {
        constexpr int m = decltype(mc)::value;
        acc[m >> 2][m & 3] = mfma(wf[m >> 2], pf[m & 3], acc[m >> 2][m & 3]);
        if constexpr (s + PD < NS && m < 8) issue1(s + PD, m);
        if constexpr (s + 1 < NS) {
          if constexpr (m == 7) wait_vm<4 + 8 * imin(PD - 1, NS - 2 - s)>();
          if constexpr (m >= 8 && m < 12) {
            tr1<((s + 1) % PD) * 4096>(tf[(s + 1) & 1].h[2 * (m - 8)], ta[m - 8]);
            tr1<((s + 1) % PD) * 4096 + 512>(tf[(s + 1) & 1].h[2 * (m - 8) + 1], ta[m - 8]);
          }
        }
        SCHED_FENCE();
      });
    });
    STAMP(1, 4);
#pragma unroll
    for (int i = 0; i < 4; ++i)
#pragma unroll
      for (int j = 0; j < 4; ++j) *(f32x4*)(ring + (i * 4 + j) * 1024 + lane * 16) = acc[j][i];
    RAW_BARRIER();
    f32x4 r[4];
#pragma unroll
    for (int q = 0; q < 4; ++q) {
      r[q] = *(const f32x4*)(lds + (w * 4 + g) * 1024 + (16 * q + c) * 16);
#pragma unroll
      for (int sw = 1; sw < 4; ++sw) r[q] += *(const f32x4*)(lds + sw * RS + (w * 4 + g) * 1024 + (16 * q + c) * 16);
    }
    u32x4 o[2];
#pragma unroll
    for (int t = 0; t < 8; ++t) {
      const u32 bb = t < 4 ? bv0[t & 3] : bv1[t & 3];
      const u32 pr = pk2(r[t >> 1][(2 * t) & 3], r[t >> 1][(2 * t + 1) & 3]);
      const u32 ps = pk2(blo(pr) + blo(bb), bhi(pr) + bhi(bb));
      const float u0 = blo(ps), u1 = bhi(ps);
      const float k0 = -2.302208198f, k1c = -0.1029432397f;
      const float e0 = __builtin_amdgcn_exp2f(u0 * (k0 + k1c * u0 * u0));
      const float e1 = __builtin_amdgcn_exp2f(u1 * (k0 + k1c * u1 * u1));
      o[t >> 2][t & 3] = pk2(u0 * __builtin_amdgcn_rcpf(1.f + e0), u1 * __builtin_amdgcn_rcpf(1.f + e1));
    }
    char* up = ws + WS_U + ((size_t)(F0 / 8 + 2 * g) * 512 + D0 + 16 * w + c) * 16;
    *(u32x4*)up = o[0];
    *(u32x4*)(up + 8192) = o[1];
    STAMP(1, 5);
  } else {
    const int h = xcd * 4 + (slot - 28);
    const rsrc_t rk = mkrs(ws + WS_KVP + (size_t)h * 65536, 65536), rq = mkrs(ws + WS_PHIQ + (size_t)h * 131072, 131072);
    u32x4 kp[16];
#pragma unroll
    for (int n = 0; n < 16; ++n) kp[n] = ld16(rk, (tid >> 7) * 32768 + (tid & 127) * 16, n * 2048);
    const f32x4 zp = *(const f32x4*)(ws + WS_ZP + (size_t)h * 4096 + tid * 16);
    u32x4 ph[32];
#pragma unroll
    for (int n = 0; n < 32; ++n) ph[n] = ld16(rq, c * 64 + g * 16, (w * 32 + n) * 1024);
    f32x4 s = __builtin_bit_cast(f32x4, kp[0]);
#pragma unroll
    for (int n = 1; n < 16; ++n) s += __builtin_bit_cast(f32x4, kp[n]);
    *(f32x4*)(lds + tid * 16) = s;
    *(f32x4*)(lds + 4096 + tid * 16) = zp;
    RAW_BARRIER();
    if (tid < 32) {
      float t = 0.f;
#pragma unroll
      for (int n = 0; n < 32; ++n) t += *(const float*)(lds + 4096 + n * 128 + tid * 4);
      *(float*)(lds + 8192 + tid * 4) = t;
    }
    RAW_BARRIER();
    STAMP(1, 6);
    u32x4 akv, az;
    {
      const int o0 = ((g >> 1) * 256 + 2 * (g & 1) * 64 + c * 4) * 4;
      const f32x4 k0 = *(const f32x4*)(lds + o0) + *(const f32x4*)(lds + 2048 + o0);
      const f32x4 k1v = *(const f32x4*)(lds + o0 + 256) + *(const f32x4*)(lds + 2048 + o0 + 256);
      akv = u32x4{pk2(k0[0], k0[1]), pk2(k0[2], k0[3]), pk2(k1v[0], k1v[1]), pk2(k1v[2], k1v[3])};
      const f32x4 z0 = *(const f32x4*)(lds + 8192 + g * 32), z1 = *(const f32x4*)(lds + 8192 + g * 32 + 16);
      az = u32x4{pk2(z0[0], z0[1]), pk2(z0[2], z0[3]), pk2(z1[0], z1[1]), pk2(z1[2], z1[3])};
    }
    char* op = ws + WS_O + ((size_t)(2 * h + (g >> 1)) * 2048 + w * 512 + c) * 16 + (g & 1) * 8;
#pragma unroll
    for (int n = 0; n < 32; ++n) {
      const f32x4 num = mfma(akv, ph[n], f32x4{0.f, 0.f, 0.f, 0.f});
      const f32x4 den = mfma(az, ph[n], f32x4{0.f, 0.f, 0.f, 0.f});
      const float rd = 1.f / rbf(rbf(den[0]) + 1e-6f);
      const u32 n0 = pk2(num[0], num[1]), n1 = pk2(num[2], num[3]);
      *(u32x2*)(op + n * 256) = u32x2{pk2(blo(n0) * rd, bhi(n0) * rd), pk2(blo(n1) * rd, bhi(n1) * rd)};
    }
    STAMP(1, 7);
    u32 pf[11];
    const int q = slot - 28;
#pragma unroll
    for (int i = 0; i < 7; ++i) { const int idx = i * 256 + tid; pf[i] = touch(w2, S_ * F_ * 2, (q * 448 + (idx >> 2)) * (S_ * 2) + xcd * 512 + (idx & 3) * 128); }
#pragma unroll
    for (int i = 0; i < 4; ++i) pf[7 + i] = touch(wo, D_ * D_ * 2, (q * 1024 + i * 256 + tid) * 128);
#pragma unroll
    for (int i = 0; i < 11; ++i) keep(pf[i]);
  }
  keep(pfh);
  STAMP_OUT(1);
}

__global__ __launch_bounds__(256, 1) void k3(const u16* __restrict__ w2, const u16* __restrict__ wo, const u16* __restrict__ b2,
                                              const char* __restrict__ ws, u16* __restrict__ out) {
  __shared__ __attribute__((aligned(16))) char lds[4 * RS];
  const int tid = threadIdx.x, lane = tid & 63, c = lane & 15, g = lane >> 4;
  const int w = __builtin_amdgcn_readfirstlane(tid >> 6);
  const int b = blockIdx.x, xcd = b & 7, slot = b >> 3;
  const int S0 = (xcd * 4 + (slot & 3)) * 64, D0 = (slot >> 2) * 64;
  const rsrc_t rw2 = mkrs(w2, F_ * S_ * 2), ru = mkrs(ws + WS_U, 1835008);
  const rsrc_t rwo = mkrs(wo, D_ * D_ * 2), ro = mkrs(ws + WS_O, 2097152);
  const int v2e = dma_voff(lane, S_ * 2, 0) + S0 * 2, v2o = dma_voff(lane, S_ * 2, 1) + S0 * 2, vu = (g * 512 + D0 + c) * 16;
  const int voe = dma_voff(lane, D_ * 2, 0) + D0 * 2, voo = dma_voff(lane, D_ * 2, 1) + D0 * 2, vo = (g * 2048 + S0 + c) * 16;
  constexpr int NY = 14, NA = 4, NS = NY + NA;
  const int sw2 = w * NY * 32 * S_ * 2, su = w * NY * 32768, swo = w * NA * 32 * D_ * 2, so = w * NA * 131072;
  STAMP_DECL;
  STAMP(2, 0);
  const u32 pf0 = touch(ws + WS_U, 1835008, (slot * 448 + tid) * 128);
  const u32 pf1 = tid < 192 ? touch(ws + WS_U, 1835008, (slot * 448 + 256 + tid) * 128)
                            : touch(ws + WS_O, 2097152, (slot * 2 + ((tid >> 5) & 1)) * 32768 + xcd * 4096 + (tid & 31) * 128);
  float gv[4];
#pragma unroll
  for (int j = 0; j < 4; ++j) gv[j] = *(const float*)(ws + WS_G + (S0 + 16 * j + c) * 4);
  const float gown = *(const float*)(ws + WS_G + (S0 + 16 * w + c) * 4);
  const float bown = blo(b2[S0 + 16 * w + c]);
  SCHED_FENCE();
  char* ring = lds + w * RS;
  u32x4 pb[PD][4];
  auto issue = [&](int s) {
    if (s < NY) {
      dma_stage(ring + (s % PD) * 4096, rw2, v2e, v2o, sw2 + s * 32 * S_ * 2, S_ * 2);
      load_p(pb[s % PD], ru, vu, su + s * 32768);
    } else {
      dma_stage(ring + (s % PD) * 4096, rwo, voe, voo, swo + (s - NY) * 32 * D_ * 2, D_ * 2);
      load_p(pb[s % PD], ro, vo, so + (s - NY) * 131072);
    }
  };
#pragma unroll
  for (int s = 0; s < PD; ++s) { issue(s); SCHED_FENCE(); }
  f32x4 ay[4][4], aa[4][4];
#pragma unroll
  for (int j = 0; j < 4; ++j)
#pragma unroll
    for (int i = 0; i < 4; ++i) { ay[j][i] = f32x4{0.f, 0.f, 0.f, 0.f}; aa[j][i] = f32x4{0.f, 0.f, 0.f, 0.f}; }
  u32 ta[4];
  tr_addr(ta, ldsaddr(ring), lane);
  STAMP(2, 1);
  {
    auto issue1 = [&](int s, int m) {
      if (s < NY) {
        if (m < 4) dma1(ring + (s % PD) * 4096 + m * 1024, rw2, (m & 1) ? v2o : v2e, sw2 + (s * 32 + m * 8) * S_ * 2);
        else pb[s % PD][m - 4] = ld16(ru, vu + (m - 4) * 256, su + s * 32768);
      } else {
        if (m < 4) dma1(ring + (s % PD) * 4096 + m * 1024, rwo, (m & 1) ? voo : voe, swo + ((s - NY) * 32 + m * 8) * D_ * 2);
        else pb[s % PD][m - 4] = ld16(ro, vo + (m - 4) * 256, so + (s - NY) * 131072);
      }
    };
    TrFrag tf[2];
    wait_vm<4 + 8 * (PD - 1)>();
    tr_issue<0>(tf[0], ta);
    static_for<0, NS>([&](auto sc) {
      constexpr int s = decltype(sc)::value;
      if (s == 1) STAMP(2, 2);
      if (s == 8) STAMP(2, 3);
      u32x4 wf[4], pf[4];
      tr_wait(tf[s & 1], wf);
#pragma unroll
      for (int i = 0; i < 4; ++i) pf[i] = pb[s % PD][i];
      static_for<0, 16>([&](auto mc) {
        constexpr int m = decltype(mc)::value;
        if constexpr (s < NY) ay[m & 3][m >> 2] = mfma(pf[m & 3], wf[m >> 2], ay[m & 3][m >> 2]);
        else aa[m >> 2][m & 3] = mfma(wf[m >> 2], pf[m & 3], aa[m >> 2][m & 3]);
        if constexpr (s + PD < NS && m < 8) issue1(s + PD, m);
        if constexpr (s + 1 < NS) {
          if constexpr (m == 7) wait_vm<4 + 8 * imin(PD - 1, NS - 2 - s)>();
          if constexpr (m >= 8 && m < 12) {
            tr1<((s + 1) % PD) * 4096>(tf[(s + 1) & 1].h[2 * (m - 8)], ta[m - 8]);
            tr1<((s + 1) % PD) * 4096 + 512>(tf[(s + 1) & 1].h[2 * (m - 8) + 1], ta[m - 8]);
          }
        }
        SCHED_FENCE();
      });
    });
  }
  STAMP(2, 4);
#pragma unroll
  for (int j = 0; j < 4; ++j) {
    const float ga = gv[j], gb = rbf(1.f - ga);
#pragma unroll
    for (int i = 0; i < 4; ++i) *(f32x4*)(ring + (j * 4 + i) * 1024 + lane * 16) = ga * aa[i][j] + gb * ay[i][j];
  }
  RAW_BARRIER();
  f32x4 r[4];
  const float t = rbf(1.f - gown) * bown;
#pragma unroll
  for (int q = 0; q < 4; ++q) {
    r[q] = *(const f32x4*)(lds + (w * 4 + g) * 1024 + (16 * q + c) * 16) + t;
#pragma unroll
    for (int sw = 1; sw < 4; ++sw) r[q] += *(const f32x4*)(lds + sw * RS + (w * 4 + g) * 1024 + (16 * q + c) * 16);
  }
  u16* op = out + (size_t)(S0 + 16 * w + c) * 512 + D0 + 16 * g;
  *(u32x4*)op = u32x4{pk2(r[0][0], r[0][1]), pk2(r[0][2], r[0][3]), pk2(r[1][0], r[1][1]), pk2(r[1][2], r[1][3])};
  *(u32x4*)(op + 8) = u32x4{pk2(r[2][0], r[2][1]), pk2(r[2][2], r[2][3]), pk2(r[3][0], r[3][1]), pk2(r[3][2], r[3][3])};
  keep(pf0); keep(pf1);
  STAMP(2, 5);
  STAMP_OUT(2);
}

extern "C" void kernel_launch(void* const* d_in, const int* in_sizes, int n_in, void* d_out, int out_size, void* d_ws,
                              size_t ws_size, hipStream_t stream) {
  const u16* x = (const u16*)d_in[0];
  const u16* wqkv = (const u16*)d_in[1];
  const u16* wphi = (const u16*)d_in[2];
  const u16* wo = (const u16*)d_in[3];
  const u16* w1 = (const u16*)d_in[5];
  const u16* b1 = (const u16*)d_in[6];
  const u16* w2 = (const u16*)d_in[7];
  const u16* b2 = (const u16*)d_in[8];
  const u16* gw = (const u16*)d_in[9];
  char* ws = (char*)d_ws;
  k1<<<256, 512, 0, stream>>>(x, wqkv, wphi, gw, w1, b1, w2, b2, wo, ws);
  k2<<<256, 256, 0, stream>>>(w1, b1, w2, wo, ws);
  k3<<<256, 256, 0, stream>>>(w2, wo, b2, ws, (u16*)d_out);
}
```

```cpp
#include <hip/hip_runtime.h>
#include <hip/hip_bf16.h>
#include <type_traits>

typedef unsigned short u16;
typedef unsigned int u32;
typedef __attribute__((ext_vector_type(8))) __bf16 bf16x8;
typedef __attribute__((ext_vector_type(2))) __bf16 bf16x2;
typedef __attribute__((ext_vector_type(4))) short s16x4;
typedef __attribute__((ext_vector_type(4))) float f32x4;
typedef __attribute__((ext_vector_type(2))) float f32x2;
typedef __attribute__((ext_vector_type(4))) unsigned int u32x4;
typedef __attribute__((ext_vector_type(2))) unsigned int u32x2;
typedef __amdgpu_buffer_rsrc_t rsrc_t;

#define DEVI __device__ __forceinline__
#define SCHED_FENCE() __builtin_amdgcn_sched_barrier(0)
#define RAW_BARRIER() do { asm volatile("s_waitcnt lgkmcnt(0)" ::: "memory"); __builtin_amdgcn_s_barrier(); asm volatile("" ::: "memory"); } while (0)

#define STAMP(k, s)
#define STAMP_DECL
#define STAMP_OUT(k)
constexpr int S_ = 2048, D_ = 512, F_ = 1792;
constexpr size_t WS_H = 0;
constexpr size_t WS_PHIQ = WS_H + 2097152;
constexpr size_t WS_KVP = WS_PHIQ + 4194304;
constexpr size_t WS_ZP = WS_KVP + 2097152;
constexpr size_t WS_G = WS_ZP + 131072;
constexpr size_t WS_U = WS_G + 8192;
constexpr size_t WS_O = WS_U + 1835008;

DEVI rsrc_t mkrs(const void* p, int bytes) { return __builtin_amdgcn_make_buffer_rsrc((void*)p, (short)0, bytes, 0x00020000); }
DEVI u32x4 ld16(rsrc_t r, int v, int s) { return __builtin_amdgcn_raw_buffer_load_b128(r, v, s, 0); }
DEVI u32x2 ld8(rsrc_t r, int v, int s) { return __builtin_amdgcn_raw_buffer_load_b64(r, v, s, 0); }
DEVI u32 pk2(float lo, float hi) { f32x2 v = {lo, hi}; return __builtin_bit_cast(u32, __builtin_convertvector(v, bf16x2)); }
DEVI float blo(u32 v) { return __builtin_bit_cast(float, v << 16); }
DEVI float bhi(u32 v) { return __builtin_bit_cast(float, v & 0xffff0000u); }
DEVI float rbf(float x) { return blo(pk2(x, x)); }
DEVI f32x4 mfma(u32x4 a, u32x4 b, f32x4 c) {
  return __builtin_amdgcn_mfma_f32_16x16x32_bf16(__builtin_bit_cast(bf16x8, a), __builtin_bit_cast(bf16x8, b), c, 0, 0, 0);
}
DEVI void wperm(const u32x2* w, u32x4* f) {
#pragma unroll
  for (int t = 0; t < 4; ++t) {
    f[0][t] = __builtin_amdgcn_perm(w[2 * t + 1].x, w[2 * t].x, 0x05040100u);
    f[1][t] = __builtin_amdgcn_perm(w[2 * t + 1].x, w[2 * t].x, 0x07060302u);
    f[2][t] = __builtin_amdgcn_perm(w[2 * t + 1].y, w[2 * t].y, 0x05040100u);
    f[3][t] = __builtin_amdgcn_perm(w[2 * t + 1].y, w[2 * t].y, 0x07060302u);
  }
}
template <int CTRL> DEVI float dppf(float x) {
  return __builtin_bit_cast(float, __builtin_amdgcn_update_dpp(0, __builtin_bit_cast(int, x), CTRL, 0xf, 0xf, true));
}
DEVI float half_sum(float x) {
  x += dppf<0xB1>(x); x += dppf<0x4E>(x); x += dppf<0x141>(x); x += dppf<0x140>(x);
  u32 b = __builtin_bit_cast(u32, x);
  auto r = __builtin_amdgcn_permlane16_swap(b, b, false, false);
  return __builtin_bit_cast(float, (u32)r[0]) + __builtin_bit_cast(float, (u32)r[1]);
}
typedef __attribute__((address_space(3))) char lchar;
DEVI u32 ldsaddr(const char* p) { return (u32)(size_t)(const lchar*)p; }
template <int N> DEVI void wait_vm() { asm volatile("s_waitcnt vmcnt(%0)" ::"n"(N) : "memory"); }
DEVI void dma_stage(char* dst, rsrc_t r, int ve, int vo, int so, int ld) {
#pragma unroll
  for (int n = 0; n < 4; ++n)
    __builtin_amdgcn_raw_ptr_buffer_load_lds(r, (lchar*)(dst + n * 1024), 16, (n & 1) ? vo : ve, so + n * 8 * ld, 0, 0);
}
DEVI int dma_voff(int lane, int ld, int odd) { return (lane >> 3) * ld + (((lane & 7) ^ ((((lane >> 4) & 1) | (odd << 1)) << 1)) << 4); }
DEVI void tr_addr(u32* ta, u32 base, int lane) {
  const int kg = lane >> 4, q = (lane >> 2) & 3, pp = lane & 3;
  const int fr = ((q >> 1) & 1) | ((kg & 1) << 1);
#pragma unroll
  for (int j = 0; j < 4; ++j) ta[j] = base + (8 * kg + q) * 128 + (((2 * j + (pp >> 1)) ^ (fr << 1)) << 4) + (pp & 1) * 8;
}
struct TrFrag { u32x2 h[8]; };
template <int OFF> DEVI void tr_issue(TrFrag& t, const u32* ta) {
  asm volatile(
      "ds_read_b64_tr_b16 %0, %8 offset:%12\n\tds_read_b64_tr_b16 %1, %8 offset:%13\n\t"
      "ds_read_b64_tr_b16 %2, %9 offset:%12\n\tds_read_b64_tr_b16 %3, %9 offset:%13\n\t"
      "ds_read_b64_tr_b16 %4, %10 offset:%12\n\tds_read_b64_tr_b16 %5, %10 offset:%13\n\t"
      "ds_read_b64_tr_b16 %6, %11 offset:%12\n\tds_read_b64_tr_b16 %7, %11 offset:%13"
      : "=&v"(t.h[0]), "=&v"(t.h[1]), "=&v"(t.h[2]), "=&v"(t.h[3]), "=&v"(t.h[4]), "=&v"(t.h[5]), "=&v"(t.h[6]), "=&v"(t.h[7])
      : "v"(ta[0]), "v"(ta[1]), "v"(ta[2]), "v"(ta[3]), "n"(OFF), "n"(OFF + 512)
      : "memory");
}
template <int N = 0> DEVI void tr_wait(TrFrag& t, u32x4* f) {
  asm volatile("s_waitcnt lgkmcnt(%8)"
               : "+v"(t.h[0]), "+v"(t.h[1]), "+v"(t.h[2]), "+v"(t.h[3]), "+v"(t.h[4]), "+v"(t.h[5]), "+v"(t.h[6]), "+v"(t.h[7])
               : "n"(N) : "memory");
#pragma unroll
  for (int j = 0; j < 4; ++j) f[j] = u32x4{t.h[2 * j].x, t.h[2 * j].y, t.h[2 * j + 1].x, t.h[2 * j + 1].y};
}
template <int OFF> DEVI void x_issue(u32x4* xf, u32 a) {
  asm volatile("ds_read_b128 %0, %4 offset:%5\n\tds_read_b128 %1, %4 offset:%6\n\tds_read_b128 %2, %4 offset:%7\n\tds_read_b128 %3, %4 offset:%8"
               : "=&v"(xf[0]), "=&v"(xf[1]), "=&v"(xf[2]), "=&v"(xf[3])
               : "v"(a), "n"(OFF), "n"(OFF + 4096), "n"(OFF + 8192), "n"(OFF + 12288) : "memory");
}
template <int OFF> DEVI void tr1(u32x2& d, u32 a) { asm volatile("ds_read_b64_tr_b16 %0, %1 offset:%2" : "=v"(d) : "v"(a), "n"(OFF) : "memory"); }
template <int OFF> DEVI void x1(u32x4& d, u32 a) { asm volatile("ds_read_b128 %0, %1 offset:%2" : "=v"(d) : "v"(a), "n"(OFF) : "memory"); }
DEVI void dma1(char* dst, rsrc_t r, int v, int so) { __builtin_amdgcn_raw_ptr_buffer_load_lds(r, (lchar*)dst, 16, v, so, 0, 0); }
DEVI u32 touch(const void* base, int bytes, int off) { return __builtin_amdgcn_raw_buffer_load_b32(mkrs(base, bytes), off, 0, 0); }
DEVI void keep(u32 v) { asm volatile("" ::"v"(v)); }
template <int I, int N, class Fn> DEVI void static_for(Fn&& f) {
  if constexpr (I < N) { f(std::integral_constant<int, I>{}); static_for<I + 1, N>(f); }
}
constexpr int imin(int a, int b) { return a < b ? a : b; }

constexpr int HS = 1152;
constexpr int L1_X = 0;
constexpr int PD1 = 6;
constexpr int CODE_BYTES = 36736;
constexpr int L1_R = 65536;
constexpr int RS1 = PD1 * 4096;
constexpr int L1_PH = L1_X;
constexpr int L1_WF = L1_R + 3 * RS1;
constexpr int L1_HT = L1_WF + 8192;
constexpr int L1_SIZE = L1_HT + 8 * HS;

__global__ __launch_bounds__(512, 1) void k1(const u16* __restrict__ x, const u16* __restrict__ wqkv,
                                              const u16* __restrict__ wphi, const u16* __restrict__ gw,
                                              const u16* __restrict__ w1, const u16* __restrict__ b1,
                                              const u16* __restrict__ w2, const u16* __restrict__ b2,
                                              const u16* __restrict__ wo, char* __restrict__ ws) {
  __shared__ __attribute__((aligned(16))) char lds[L1_SIZE];
  const int tid = threadIdx.x, lane = tid & 63, c = lane & 15, g = lane >> 4;
  const int w = __builtin_amdgcn_readfirstlane(tid >> 6);
  const int b = blockIdx.x, xcd = b & 7, slot = b >> 3;
  const int rb = (xcd >> 1) * 8 + (slot & 7), hg = (xcd & 1) * 4 + (slot >> 3);

  STAMP_DECL;
  STAMP(0, 0);
  u32 pf[6] = {0u, 0u, 0u, 0u, 0u, 0u};
  if (w < 3) {
    const rsrc_t rw = mkrs(wqkv, 512 * 1536 * 2);
    const int cb = (w * 512 + hg * 64) * 2;
    const int ve = dma_voff(lane, 3072, 0) + cb, vo = dma_voff(lane, 3072, 1) + cb;
    char* ring = lds + L1_R + w * RS1;
#pragma unroll
    for (int s = 0; s < 4; ++s) dma_stage(ring + s * 4096, rw, ve, vo, s * 32 * 3072, 3072);
    f32x4 acc[4][4];
#pragma unroll
    for (int j = 0; j < 4; ++j)
#pragma unroll
      for (int t = 0; t < 4; ++t) acc[j][t] = f32x4{0.f, 0.f, 0.f, 0.f};
    u32 xa[4], ta[4];
#pragma unroll
    for (int t = 0; t < 4; ++t) xa[t] = ldsaddr(lds) + c * 256 + (((4 * t + g) ^ c) << 4);
    tr_addr(ta, ldsaddr(ring), lane);
    STAMP(0, 1);
    RAW_BARRIER();
    STAMP(0, 2);
    TrFrag tf[3];
    u32x4 xf[3][4];
    wait_vm<12>();
    tr_issue<0>(tf[0], ta);
    x_issue<0>(xf[0], xa[0]);
    wait_vm<8>();
    tr_issue<4096>(tf[1], ta);
    x_issue<0>(xf[1], xa[1]);
    static_for<0, 16>([&](auto sc) {
      constexpr int s = decltype(sc)::value, n = s + 2;
      u32x4 wf[4];
      tr_wait<(s < 15 ? 12 : 0)>(tf[s % 3], wf);
      asm volatile("" : "+v"(xf[s % 3][0]), "+v"(xf[s % 3][1]), "+v"(xf[s % 3][2]), "+v"(xf[s % 3][3]));
      static_for<0, 16>([&](auto mc) {
        constexpr int m = decltype(mc)::value, mm = m & 3;
        acc[m >> 2][mm] = mfma(wf[m >> 2], xf[s % 3][mm], acc[m >> 2][mm]);
        constexpr int st = s < 2 ? 4 + 2 * s + (m >= 12) : s + PD1;
        if constexpr (st < 16 && (m < 4 || (m >= 12 && s < 2)))
          dma1(ring + (st % PD1) * 4096 + mm * 1024, rw, (m & 1) ? vo : ve, (st * 32 + mm * 8) * 3072);
        if constexpr (s == 10 && m < 3) {
          const int idx = (w * 3 + mm) * 64 + lane, r = idx / 7;
          pf[mm] = touch(w1, S_ * F_ * 2, idx < 448 ? (slot * 64 + r) * (F_ * 2) + (xcd >> 1) * 896 + (idx - r * 7) * 128 : 0x7fffffff);
        }
        if constexpr (n < 16) {
          if constexpr (m == 3 && n == 8) { __builtin_amdgcn_s_barrier(); STAMP(0, 3); }
          if constexpr (m >= 4 && m < 8) x1<(n >> 2) * 16384 + mm * 4096>(xf[n % 3][mm], xa[n & 3]);
          if constexpr (m == 7) wait_vm<4 * imin(s + 2, imin(4, 13 - s)) + (s >= 10 ? 3 : 0)>();
          if constexpr (m >= 8 && m < 12) {
            tr1<(n % PD1) * 4096>(tf[n % 3].h[2 * mm], ta[mm]);
            tr1<(n % PD1) * 4096 + 512>(tf[n % 3].h[2 * mm + 1], ta[mm]);
          }
        }
        SCHED_FENCE();
      });
    });
#pragma unroll
    for (int t = 0; t < 4; ++t)
#pragma unroll
      for (int j = 0; j < 4; ++j)
        *(u32x2*)(ring + (t * 2 + (g >> 1)) * HS + (16 * j + c) * 16 + (g & 1) * 8) =
            u32x2{pk2(acc[j][t][0], acc[j][t][1]), pk2(acc[j][t][2], acc[j][t][3])};
  } else if (w == 3) {
#pragma unroll
    for (int i = 0; i < 3; ++i) {
      const int idx = i * 64 + lane, r = idx / 3;
      pf[i] = touch(wqkv, 512 * 1536 * 2, idx < 144 ? (128 + (slot & 7) * 48 + r) * 3072 + (idx - r * 3) * 1024 + hg * 128 : 0x7fffffff);
    }
    pf[3] = touch((const void*)(__builtin_amdgcn_s_getpc() & ~127ull), CODE_BYTES, lane < 12 ? (slot * 12 + lane) * 128 : 0x7fffffff);
    RAW_BARRIER();
    u32x2 wp[16];
    const rsrc_t rp = mkrs(wphi, 32 * 256 * 2);
    const int voff = ((4 * hg + g) * 256 + 4 * (c & 3)) * 2;
#pragma unroll
    for (int d = 0; d < 16; ++d) wp[d] = ld8(rp, voff + d * 32, 0);
    pf[4] = touch(b1, F_ * 2, lane * 128);
    pf[5] = touch(b2, S_ * 2, lane * 128);
    RAW_BARRIER();
    const bool on = (g == (c >> 2));
#pragma unroll
    for (int h = 0; h < 2; ++h) {
      u32x4 f[4];
      wperm(wp + 8 * h, f);
#pragma unroll
      for (int jj = 0; jj < 4; ++jj) {
        u32x4 v = on ? f[jj] : u32x4{0u, 0u, 0u, 0u};
        *(u32x4*)(lds + L1_WF + (jj * 2 + h) * 1024 + lane * 16) = v;
      }
    }
  } else {
    const rsrc_t rx = mkrs(x, S_ * D_ * 2);
    const int kq0 = w & 2;
#pragma unroll
    for (int kq = 0; kq < 2; ++kq)
#pragma unroll
      for (int i = 0; i < 8; ++i) {
        const int pg = (w & 1) * 8 + i;
        dma1(lds + L1_X + (kq0 + kq) * 16384 + pg * 1024, rx, g * 1024 + ((c ^ ((4 * (i & 3) + g) & 15)) << 4), (rb * 64 + 4 * pg) * 1024 + (kq0 + kq) * 256);
      }
    const int hl = lane & 31;
    const u32x4 gv0 = *(const u32x4*)(gw + hl * 8), gv1 = *(const u32x4*)(gw + 256 + hl * 8);
    SCHED_FENCE();
    if (w < 6) wait_vm<2>();
    RAW_BARRIER();
    wait_vm<0>();
    RAW_BARRIER();
    const int p = 8 * hg + 2 * (w - 4) + (lane >> 5);
    const char* xp = lds + L1_X + (g & 1) * 16384 + p * 256 + ((c ^ (p & 15)) << 4);
    const u32x4 v0 = *(const u32x4*)xp, v1 = *(const u32x4*)(xp + 32768);
    float xv[16], sum = 0.f, gs = 0.f;
#pragma unroll
    for (int e = 0; e < 8; ++e) {
      const u32 v = e < 4 ? v0[e & 3] : v1[e & 3], gg = e < 4 ? gv0[e & 3] : gv1[e & 3];
      xv[2 * e] = blo(v); xv[2 * e + 1] = bhi(v);
      sum += xv[2 * e] + xv[2 * e + 1];
      const u32 pr = pk2(xv[2 * e] * blo(gg), xv[2 * e + 1] * bhi(gg));
      gs += blo(pr) + bhi(pr);
    }
    sum = half_sum(sum) * (1.f / 512.f);
    gs = half_sum(gs);
    float var = 0.f;
#pragma unroll
    for (int e = 0; e < 16; ++e) { const float d = xv[e] - sum; var += d * d; }
    var = half_sum(var) * (1.f / 512.f);
    const float mean = rbf(sum);
    const float rs = 1.f / rbf(sqrtf(rbf(rbf(var) + 1e-5f)));
    u32 nn[8];
#pragma unroll
    for (int e = 0; e < 8; ++e) {
      const u32 pr = pk2(xv[2 * e] - mean, xv[2 * e + 1] - mean);
      nn[e] = pk2(blo(pr) * rs, bhi(pr) * rs);
    }
    char* hp = lds + L1_HT + lane * 16 + (w - 4) * 4;
#pragma unroll
    for (int t = 0; t < 4; ++t) {
      const auto q = __builtin_amdgcn_permlane32_swap(nn[t], nn[4 + t], false, false);
      *(u32*)(hp + (2 * t) * HS) = __builtin_amdgcn_perm((u32)q[1], (u32)q[0], 0x05040100u);
      *(u32*)(hp + (2 * t + 1) * HS) = __builtin_amdgcn_perm((u32)q[1], (u32)q[0], 0x07060302u);
    }
    if (hl == 0) *(float*)(ws + WS_G + (rb * 64 + p) * 4) = rbf(1.f / (1.f + __expf(-rbf(gs))));
  }
  STAMP(0, 4);
  RAW_BARRIER();
  STAMP(0, 5);
  *(u32x4*)(ws + WS_H + (size_t)(rb * 8 + hg) * 8192 + tid * 16) = *(const u32x4*)(lds + L1_HT + (lane & 7) * HS + (tid >> 3) * 16);
  {
    const int mat = w >> 2, tau = w & 3;
    const u32x4 b0 = *(const u32x4*)(lds + L1_R + mat * RS1 + (tau * 2) * HS + lane * 16);
    const u32x4 b1 = *(const u32x4*)(lds + L1_R + mat * RS1 + (tau * 2 + 1) * HS + lane * 16);
    float p[16];
#pragma unroll
    for (int jj = 0; jj < 4; ++jj) {
      const u32x4 a0 = *(const u32x4*)(lds + L1_WF + (jj * 2) * 1024 + lane * 16);
      const u32x4 a1 = *(const u32x4*)(lds + L1_WF + (jj * 2 + 1) * 1024 + lane * 16);
      f32x4 r = mfma(a1, b1, mfma(a0, b0, f32x4{0.f, 0.f, 0.f, 0.f}));
      const u32 u0 = pk2(r[0], r[1]), u1 = pk2(r[2], r[3]);
      p[jj] = blo(u0); p[4 + jj] = bhi(u0); p[8 + jj] = blo(u1); p[12 + jj] = bhi(u1);
    }
    float mx = p[0], mn = p[0];
#pragma unroll
    for (int f = 1; f < 16; ++f) { mx = fmaxf(mx, p[f]); mn = fminf(mn, p[f]); }
    const float L2E = 1.4426950408889634f;
    float ep[16], en[16], sp = 0.f, sn = 0.f;
#pragma unroll
    for (int f = 0; f < 16; ++f) {
      ep[f] = __builtin_amdgcn_exp2f((p[f] - mx) * L2E);
      en[f] = __builtin_amdgcn_exp2f((mn - p[f]) * L2E);
      sp += ep[f]; sn += en[f];
    }
    const float rp = __builtin_amdgcn_rcpf(sp), rn = __builtin_amdgcn_rcpf(sn);
    u32x4 ph[4];
#pragma unroll
    for (int t = 0; t < 4; ++t) {
      ph[0][t] = pk2(ep[2 * t] * rp, ep[2 * t + 1] * rp);
      ph[1][t] = pk2(ep[8 + 2 * t] * rp, ep[9 + 2 * t] * rp);
      ph[2][t] = pk2(en[2 * t] * rn, en[2 * t + 1] * rn);
      ph[3][t] = pk2(en[8 + 2 * t] * rn, en[9 + 2 * t] * rn);
    }
    if (mat == 0) {
      char* dst = ws + WS_PHIQ + ((size_t)(4 * hg + g) * 2048 + rb * 64 + 16 * tau + c) * 64;
#pragma unroll
      for (int pc = 0; pc < 4; ++pc) *(u32x4*)(dst + pc * 16) = ph[pc];
    } else {
#pragma unroll
      for (int pc = 0; pc < 4; ++pc) *(u32x4*)(lds + L1_PH + (tau * 4 + pc) * HS + lane * 16) = ph[pc];
    }
  }
  STAMP(0, 6);
  RAW_BARRIER();
  {
    const int hh = w & 3, sign = w >> 2;
    const int q = (lane >> 2) & 3, pp = lane & 3;
    const int ro = (pp >> 1) * HS + (16 * hh + 4 * g + q) * 16 + (pp & 1) * 8;
    f32x4 kv = {0.f, 0.f, 0.f, 0.f}, z = {0.f, 0.f, 0.f, 0.f};
    const u32x4 ones = {0x3f803f80u, 0x3f803f80u, 0x3f803f80u, 0x3f803f80u};
#pragma unroll
    for (int sg = 0; sg < 2; ++sg) {
      u32x2 a0, a1, v0, v1;
      const u32 pa = ldsaddr(lds + L1_PH) + (8 * sg + 2 * sign) * HS + ro, pv = ldsaddr(lds + L1_R + 2 * RS1) + 4 * sg * HS + ro;
      asm volatile("ds_read_b64_tr_b16 %0, %4\n\tds_read_b64_tr_b16 %1, %4 offset:%6\n\tds_read_b64_tr_b16 %2, %5\n\t"
                   "ds_read_b64_tr_b16 %3, %5 offset:%7\n\ts_waitcnt lgkmcnt(0)"
                   : "=&v"(a0), "=&v"(a1), "=&v"(v0), "=&v"(v1) : "v"(pa), "v"(pv), "n"(4 * HS), "n"(2 * HS) : "memory");
      const u32x4 a = {a0.x, a0.y, a1.x, a1.y}, v = {v0.x, v0.y, v1.x, v1.y};
      kv = mfma(a, v, kv);
      z = mfma(a, ones, z);
    }
    const int hb = (4 * hg + hh) * 32 + rb;
    *(f32x4*)(ws + WS_KVP + ((size_t)hb * 512 + sign * 256 + lane * 4) * 4) = kv;
    if (c == 0) *(f32x4*)(ws + WS_ZP + ((size_t)hb * 32 + sign * 16 + 4 * g) * 4) = z;
  }
#pragma unroll
  for (int i = 0; i < 6; ++i) keep(pf[i]);
  STAMP(0, 7);
  STAMP_OUT(0);
}

constexpr int PD = 6;
constexpr int RS = PD * 4096;
DEVI void load_p(u32x4* p, rsrc_t rp, int vp, int sp) {
#pragma unroll
  for (int i = 0; i < 4; ++i) p[i] = ld16(rp, vp + i * 256, sp);
}

__global__ __launch_bounds__(256, 1) void k2(const u16* __restrict__ w1, const u16* __restrict__ b1, const u16* __restrict__ w2,
                                              const u16* __restrict__ wo, char* __restrict__ ws) {
  __shared__ __attribute__((aligned(16))) char lds[4 * RS];
  const int tid = threadIdx.x, lane = tid & 63, c = lane & 15, g = lane >> 4;
  const int w = __builtin_amdgcn_readfirstlane(tid >> 6);
  const int b = blockIdx.x, xcd = b & 7, slot = b >> 3;
  STAMP_DECL;
  STAMP(1, 0);
  if (slot < 28) {
    const int D0 = ((xcd & 1) * 4 + (slot & 3)) * 64, F0 = ((xcd >> 1) * 7 + (slot >> 2)) * 64;
    const rsrc_t rw = mkrs(w1, S_ * F_ * 2), rp = mkrs(ws + WS_H, 2097152);
    const int ve = dma_voff(lane, F_ * 2, 0) + F0 * 2, vo = dma_voff(lane, F_ * 2, 1) + F0 * 2, vp = (g * 512 + D0 + c) * 16;
    constexpr int NS = 16;
    const int sw0 = w * NS * 32 * F_ * 2, sp0 = w * NS * 32768;
    const u32x4 bv0 = *(const u32x4*)(b1 + F0 + 16 * g), bv1 = *(const u32x4*)(b1 + F0 + 16 * g + 8);
    SCHED_FENCE();
    char* ring = lds + w * RS;
    u32x4 pb[PD][4];
#pragma unroll
    for (int s = 0; s < PD; ++s) {
      dma_stage(ring + s * 4096, rw, ve, vo, sw0 + s * 32 * F_ * 2, F_ * 2);
      load_p(pb[s], rp, vp, sp0 + s * 32768);
      SCHED_FENCE();
    }
    f32x4 acc[4][4];
#pragma unroll
    for (int j = 0; j < 4; ++j)
#pragma unroll
      for (int i = 0; i < 4; ++i) acc[j][i] = f32x4{0.f, 0.f, 0.f, 0.f};
    u32 ta[4];
    tr_addr(ta, ldsaddr(ring), lane);
    STAMP(1, 1);
    auto issue1 = [&](int s, int m) {
      if (m < 4) dma1(ring + (s % PD) * 4096 + m * 1024, rw, (m & 1) ? vo : ve, sw0 + (s * 32 + m * 8) * F_ * 2);
      else pb[s % PD][m - 4] = ld16(rp, vp + (m - 4) * 256, sp0 + s * 32768);
    };
    TrFrag tf[2];
    u32 t0, t1;
    wait_vm<4 + 8 * (PD - 1)>();
    tr_issue<0>(tf[0], ta);
    static_for<0, NS>([&](auto sc) {
      constexpr int s = decltype(sc)::value;
      if (s == 1) STAMP(1, 2);
      if (s == 8) STAMP(1, 3);
      u32x4 wf[4], pf[4];
      tr_wait(tf[s & 1], wf);
#pragma unroll
      for (int i = 0; i < 4; ++i) pf[i] = pb[s % PD][i];
      static_for<0, 16>([&](auto mc) {
        constexpr int m = decltype(mc)::value;
        acc[m >> 2][m & 3] = mfma(wf[m >> 2], pf[m & 3], acc[m >> 2][m & 3]);
        if constexpr (s + PD < NS && m < 8) issue1(s + PD, m);
        if constexpr (s + PD == NS && m == 0) {
          t0 = touch(w2, S_ * F_ * 2, (slot * 64 + (tid >> 2)) * (S_ * 2) + xcd * 512 + (tid & 3) * 128);
          t1 = touch(wo, D_ * D_ * 2, lane < 37 ? (slot * 148 + w * 37 + lane) * 128 : 0x7fffffff);
        }
        if constexpr (s + 1 < NS) {
          if constexpr (m == 7) wait_vm<4 + 8 * imin(PD - 1, NS - 2 - s) + (s + PD >= NS ? 2 : 0)>();
          if constexpr (m >= 8 && m < 12) {
            tr1<((s + 1) % PD) * 4096>(tf[(s + 1) & 1].h[2 * (m - 8)], ta[m - 8]);
            tr1<((s + 1) % PD) * 4096 + 512>(tf[(s + 1) & 1].h[2 * (m - 8) + 1], ta[m - 8]);
          }
        }
        SCHED_FENCE();
      });
    });
    STAMP(1, 4);
#pragma unroll
    for (int i = 0; i < 4; ++i)
#pragma unroll
      for (int j = 0; j < 4; ++j) *(f32x4*)(ring + (i * 4 + j) * 1024 + lane * 16) = acc[j][i];
    RAW_BARRIER();
    f32x4 r[4];
#pragma unroll
    for (int q = 0; q < 4; ++q) {
      r[q] = *(const f32x4*)(lds + (w * 4 + g) * 1024 + (16 * q + c) * 16);
#pragma unroll
      for (int sw = 1; sw < 4; ++sw) r[q] += *(const f32x4*)(lds + sw * RS + (w * 4 + g) * 1024 + (16 * q + c) * 16);
    }
    u32x4 o[2];
#pragma unroll
    for (int t = 0; t < 8; ++t) {
      const u32 bb = t < 4 ? bv0[t & 3] : bv1[t & 3];
      const u32 pr = pk2(r[t >> 1][(2 * t) & 3], r[t >> 1][(2 * t + 1) & 3]);
      const u32 ps = pk2(blo(pr) + blo(bb), bhi(pr) + bhi(bb));
      const float u0 = blo(ps), u1 = bhi(ps);
      const float k0 = -2.302208198f, k1c = -0.1029432397f;
      const float e0 = __builtin_amdgcn_exp2f(u0 * (k0 + k1c * u0 * u0));
      const float e1 = __builtin_amdgcn_exp2f(u1 * (k0 + k1c * u1 * u1));
      o[t >> 2][t & 3] = pk2(u0 * __builtin_amdgcn_rcpf(1.f + e0), u1 * __builtin_amdgcn_rcpf(1.f + e1));
    }
    char* up = ws + WS_U + ((size_t)(F0 / 8 + 2 * g) * 512 + D0 + 16 * w + c) * 16;
    *(u32x4*)up = o[0];
    *(u32x4*)(up + 8192) = o[1];
    STAMP(1, 5);
    keep(t0); keep(t1);
  } else {
    const int h = xcd * 4 + (slot - 28);
    const rsrc_t rk = mkrs(ws + WS_KVP + (size_t)h * 65536, 65536), rq = mkrs(ws + WS_PHIQ + (size_t)h * 131072, 131072);
    u32x4 kp[16];
#pragma unroll
    for (int n = 0; n < 16; ++n) kp[n] = ld16(rk, (tid >> 7) * 32768 + (tid & 127) * 16, n * 2048);
    const f32x4 zp = *(const f32x4*)(ws + WS_ZP + (size_t)h * 4096 + tid * 16);
    u32x4 ph[32];
#pragma unroll
    for (int n = 0; n < 32; ++n) ph[n] = ld16(rq, c * 64 + g * 16, (w * 32 + n) * 1024);
    f32x4 s = __builtin_bit_cast(f32x4, kp[0]);
#pragma unroll
    for (int n = 1; n < 16; ++n) s += __builtin_bit_cast(f32x4, kp[n]);
    *(f32x4*)(lds + tid * 16) = s;
    *(f32x4*)(lds + 4096 + tid * 16) = zp;
    RAW_BARRIER();
    if (tid < 32) {
      float t = 0.f;
#pragma unroll
      for (int n = 0; n < 32; ++n) t += *(const float*)(lds + 4096 + n * 128 + tid * 4);
      *(float*)(lds + 8192 + tid * 4) = t;
    }
    RAW_BARRIER();
    STAMP(1, 6);
    u32x4 akv, az;
    {
      const int o0 = ((g >> 1) * 256 + 2 * (g & 1) * 64 + c * 4) * 4;
      const f32x4 k0 = *(const f32x4*)(lds + o0) + *(const f32x4*)(lds + 2048 + o0);
      const f32x4 k1v = *(const f32x4*)(lds + o0 + 256) + *(const f32x4*)(lds + 2048 + o0 + 256);
      akv = u32x4{pk2(k0[0], k0[1]), pk2(k0[2], k0[3]), pk2(k1v[0], k1v[1]), pk2(k1v[2], k1v[3])};
      const f32x4 z0 = *(const f32x4*)(lds + 8192 + g * 32), z1 = *(const f32x4*)(lds + 8192 + g * 32 + 16);
      az = u32x4{pk2(z0[0], z0[1]), pk2(z0[2], z0[3]), pk2(z1[0], z1[1]), pk2(z1[2], z1[3])};
    }
    char* op = ws + WS_O + ((size_t)(2 * h + (g >> 1)) * 2048 + w * 512 + c) * 16 + (g & 1) * 8;
#pragma unroll
    for (int n = 0; n < 32; ++n) {
      const f32x4 num = mfma(akv, ph[n], f32x4{0.f, 0.f, 0.f, 0.f});
      const f32x4 den = mfma(az, ph[n], f32x4{0.f, 0.f, 0.f, 0.f});
      const float rd = 1.f / rbf(rbf(den[0]) + 1e-6f);
      const u32 n0 = pk2(num[0], num[1]), n1 = pk2(num[2], num[3]);
      *(u32x2*)(op + n * 256) = u32x2{pk2(blo(n0) * rd, bhi(n0) * rd), pk2(blo(n1) * rd, bhi(n1) * rd)};
    }
    STAMP(1, 7);
  }
  STAMP_OUT(1);
}

__global__ __launch_bounds__(256, 1) void k3(const u16* __restrict__ w2, const u16* __restrict__ wo, const u16* __restrict__ b2,
                                              const char* __restrict__ ws, u16* __restrict__ out) {
  __shared__ __attribute__((aligned(16))) char lds[4 * RS];
  const int tid = threadIdx.x, lane = tid & 63, c = lane & 15, g = lane >> 4;
  const int w = __builtin_amdgcn_readfirstlane(tid >> 6);
  const int b = blockIdx.x, xcd = b & 7, slot = b >> 3;
  const int S0 = (xcd * 4 + (slot & 3)) * 64, D0 = (slot >> 2) * 64;
  const rsrc_t rw2 = mkrs(w2, F_ * S_ * 2), ru = mkrs(ws + WS_U, 1835008);
  const rsrc_t rwo = mkrs(wo, D_ * D_ * 2), ro = mkrs(ws + WS_O, 2097152);
  const int v2e = dma_voff(lane, S_ * 2, 0) + S0 * 2, v2o = dma_voff(lane, S_ * 2, 1) + S0 * 2, vu = (g * 512 + D0 + c) * 16;
  const int voe = dma_voff(lane, D_ * 2, 0) + D0 * 2, voo = dma_voff(lane, D_ * 2, 1) + D0 * 2, vo = (g * 2048 + S0 + c) * 16;
  constexpr int NY = 14, NA = 4, NS = NY + NA;
  const int sw2 = w * NY * 32 * S_ * 2, su = w * NY * 32768, swo = w * NA * 32 * D_ * 2, so = w * NA * 131072;
  STAMP_DECL;
  STAMP(2, 0);
  float gv[4];
#pragma unroll
  for (int j = 0; j < 4; ++j) gv[j] = *(const float*)(ws + WS_G + (S0 + 16 * j + c) * 4);
  const float gown = *(const float*)(ws + WS_G + (S0 + 16 * w + c) * 4);
  const float bown = blo(b2[S0 + 16 * w + c]);
  SCHED_FENCE();
  char* ring = lds + w * RS;
  u32x4 pb[PD][4];
  auto issue = [&](int s) {
    if (s < NY) {
      dma_stage(ring + (s % PD) * 4096, rw2, v2e, v2o, sw2 + s * 32 * S_ * 2, S_ * 2);
      load_p(pb[s % PD], ru, vu, su + s * 32768);
    } else {
      dma_stage(ring + (s % PD) * 4096, rwo, voe, voo, swo + (s - NY) * 32 * D_ * 2, D_ * 2);
      load_p(pb[s % PD], ro, vo, so + (s - NY) * 131072);
    }
  };
#pragma unroll
  for (int s = 0; s < PD; ++s) { issue(s); SCHED_FENCE(); }
  f32x4 ay[4][4], aa[4][4];
#pragma unroll
  for (int j = 0; j < 4; ++j)
#pragma unroll
    for (int i = 0; i < 4; ++i) { ay[j][i] = f32x4{0.f, 0.f, 0.f, 0.f}; aa[j][i] = f32x4{0.f, 0.f, 0.f, 0.f}; }
  u32 ta[4];
  tr_addr(ta, ldsaddr(ring), lane);
  STAMP(2, 1);
  {
    auto issue1 = [&](int s, int m) {
      if (s < NY) {
        if (m < 4) dma1(ring + (s % PD) * 4096 + m * 1024, rw2, (m & 1) ? v2o : v2e, sw2 + (s * 32 + m * 8) * S_ * 2);
        else pb[s % PD][m - 4] = ld16(ru, vu + (m - 4) * 256, su + s * 32768);
      } else {
        if (m < 4) dma1(ring + (s % PD) * 4096 + m * 1024, rwo, (m & 1) ? voo : voe, swo + ((s - NY) * 32 + m * 8) * D_ * 2);
        else pb[s % PD][m - 4] = ld16(ro, vo + (m - 4) * 256, so + (s - NY) * 131072);
      }
    };
    TrFrag tf[2];
    wait_vm<4 + 8 * (PD - 1)>();
    tr_issue<0>(tf[0], ta);
    static_for<0, NS>([&](auto sc) {
      constexpr int s = decltype(sc)::value;
      if (s == 1) STAMP(2, 2);
      if (s == 8) STAMP(2, 3);
      u32x4 wf[4], pf[4];
      tr_wait(tf[s & 1], wf);
#pragma unroll
      for (int i = 0; i < 4; ++i) pf[i] = pb[s % PD][i];
      static_for<0, 16>([&](auto mc) {
        constexpr int m = decltype(mc)::value;
        if constexpr (s < NY) ay[m & 3][m >> 2] = mfma(pf[m & 3], wf[m >> 2], ay[m & 3][m >> 2]);
        else aa[m >> 2][m & 3] = mfma(wf[m >> 2], pf[m & 3], aa[m >> 2][m & 3]);
        if constexpr (s + PD < NS && m < 8) issue1(s + PD, m);
        if constexpr (s + 1 < NS) {
          if constexpr (m == 7) wait_vm<4 + 8 * imin(PD - 1, NS - 2 - s)>();
          if constexpr (m >= 8 && m < 12) {
            tr1<((s + 1) % PD) * 4096>(tf[(s + 1) & 1].h[2 * (m - 8)], ta[m - 8]);
            tr1<((s + 1) % PD) * 4096 + 512>(tf[(s + 1) & 1].h[2 * (m - 8) + 1], ta[m - 8]);
          }
        }
        SCHED_FENCE();
      });
    });
  }
  STAMP(2, 4);
#pragma unroll
  for (int j = 0; j < 4; ++j) {
    const float ga = gv[j], gb = rbf(1.f - ga);
#pragma unroll
    for (int i = 0; i < 4; ++i) *(f32x4*)(ring + (j * 4 + i) * 1024 + lane * 16) = ga * aa[i][j] + gb * ay[i][j];
  }
  RAW_BARRIER();
  f32x4 r[4];
  const float t = rbf(1.f - gown) * bown;
#pragma unroll
  for (int q = 0; q < 4; ++q) {
    r[q] = *(const f32x4*)(lds + (w * 4 + g) * 1024 + (16 * q + c) * 16) + t;
#pragma unroll
    for (int sw = 1; sw < 4; ++sw) r[q] += *(const f32x4*)(lds + sw * RS + (w * 4 + g) * 1024 + (16 * q + c) * 16);
  }
  u16* op = out + (size_t)(S0 + 16 * w + c) * 512 + D0 + 16 * g;
  *(u32x4*)op = u32x4{pk2(r[0][0], r[0][1]), pk2(r[0][2], r[0][3]), pk2(r[1][0], r[1][1]), pk2(r[1][2], r[1][3])};
  *(u32x4*)(op + 8) = u32x4{pk2(r[2][0], r[2][1]), pk2(r[2][2], r[2][3]), pk2(r[3][0], r[3][1]), pk2(r[3][2], r[3][3])};
  STAMP(2, 5);
  STAMP_OUT(2);
}

extern "C" void kernel_launch(void* const* d_in, const int* in_sizes, int n_in, void* d_out, int out_size, void* d_ws,
                              size_t ws_size, hipStream_t stream) {
  const u16* x = (const u16*)d_in[0];
  const u16* wqkv = (const u16*)d_in[1];
  const u16* wphi = (const u16*)d_in[2];
  const u16* wo = (const u16*)d_in[3];
  const u16* w1 = (const u16*)d_in[5];
  const u16* b1 = (const u16*)d_in[6];
  const u16* w2 = (const u16*)d_in[7];
  const u16* b2 = (const u16*)d_in[8];
  const u16* gw = (const u16*)d_in[9];
  char* ws = (char*)d_ws;
  k1<<<256, 512, 0, stream>>>(x, wqkv, wphi, gw, w1, b1, w2, b2, wo, ws);
  k2<<<256, 256, 0, stream>>>(w1, b1, w2, wo, ws);
  k3<<<256, 256, 0, stream>>>(w2, wo, b2, ws, (u16*)d_out);
}
```

```cpp
#include <hip/hip_runtime.h>
#include <hip/hip_bf16.h>
#include <type_traits>

typedef unsigned short u16;
typedef unsigned int u32;
typedef __attribute__((ext_vector_type(8))) __bf16 bf16x8;
typedef __attribute__((ext_vector_type(2))) __bf16 bf16x2;
typedef __attribute__((ext_vector_type(4))) short s16x4;
typedef __attribute__((ext_vector_type(4))) float f32x4;
typedef __attribute__((ext_vector_type(2))) float f32x2;
typedef __attribute__((ext_vector_type(4))) unsigned int u32x4;
typedef __attribute__((ext_vector_type(2))) unsigned int u32x2;
typedef __amdgpu_buffer_rsrc_t rsrc_t;

#define DEVI __device__ __forceinline__
#define SCHED_FENCE() __builtin_amdgcn_sched_barrier(0)
#define RAW_BARRIER() do { asm volatile("s_waitcnt lgkmcnt(0)" ::: "memory"); __builtin_amdgcn_s_barrier(); asm volatile("" ::: "memory"); } while (0)

#define STAMP(k, s)
#define STAMP_DECL
#define STAMP_OUT(k)
#define W2K1 0
#define STAUX 17
#define W2O 0
#define OUTAUX 0
#define TS 12
constexpr int S_ = 2048, D_ = 512, F_ = 1792;
constexpr size_t WS_H = 0;
constexpr size_t WS_PHIQ = WS_H + 2097152;
constexpr size_t WS_KVP = WS_PHIQ + 4194304;
constexpr size_t WS_ZP = WS_KVP + 2097152;
constexpr size_t WS_G = WS_ZP + 131072;
constexpr size_t WS_U = WS_G + 8192;
constexpr size_t WS_O = WS_U + 1835008;

DEVI rsrc_t mkrs(const void* p, int bytes) { return __builtin_amdgcn_make_buffer_rsrc((void*)p, (short)0, bytes, 0x00020000); }
DEVI u32x4 ld16(rsrc_t r, int v, int s) { return __builtin_amdgcn_raw_buffer_load_b128(r, v, s, 0); }
DEVI u32x2 ld8(rsrc_t r, int v, int s) { return __builtin_amdgcn_raw_buffer_load_b64(r, v, s, 0); }
constexpr int WS_END = (int)WS_O + 2097152;
DEVI void st16(char* ws, int off, u32x4 v) { __builtin_amdgcn_raw_buffer_store_b128(v, mkrs(ws, WS_END), off, 0, STAUX); }
DEVI void st8(char* ws, int off, u32x2 v) { __builtin_amdgcn_raw_buffer_store_b64(v, mkrs(ws, WS_END), off, 0, STAUX); }
DEVI u32 pk2(float lo, float hi) { f32x2 v = {lo, hi}; return __builtin_bit_cast(u32, __builtin_convertvector(v, bf16x2)); }
DEVI float blo(u32 v) { return __builtin_bit_cast(float, v << 16); }
DEVI float bhi(u32 v) { return __builtin_bit_cast(float, v & 0xffff0000u); }
DEVI float rbf(float x) { return blo(pk2(x, x)); }
DEVI f32x4 mfma(u32x4 a, u32x4 b, f32x4 c) {
  return __builtin_amdgcn_mfma_f32_16x16x32_bf16(__builtin_bit_cast(bf16x8, a), __builtin_bit_cast(bf16x8, b), c, 0, 0, 0);
}
DEVI void wperm(const u32x2* w, u32x4* f) {
#pragma unroll
  for (int t = 0; t < 4; ++t) {
    f[0][t] = __builtin_amdgcn_perm(w[2 * t + 1].x, w[2 * t].x, 0x05040100u);
    f[1][t] = __builtin_amdgcn_perm(w[2 * t + 1].x, w[2 * t].x, 0x07060302u);
    f[2][t] = __builtin_amdgcn_perm(w[2 * t + 1].y, w[2 * t].y, 0x05040100u);
    f[3][t] = __builtin_amdgcn_perm(w[2 * t + 1].y, w[2 * t].y, 0x07060302u);
  }
}
template <int CTRL> DEVI float dppf(float x) {
  return __builtin_bit_cast(float, __builtin_amdgcn_update_dpp(0, __builtin_bit_cast(int, x), CTRL, 0xf, 0xf, true));
}
DEVI float half_sum(float x) {
  x += dppf<0xB1>(x); x += dppf<0x4E>(x); x += dppf<0x141>(x); x += dppf<0x140>(x);
  u32 b = __builtin_bit_cast(u32, x);
  auto r = __builtin_amdgcn_permlane16_swap(b, b, false, false);
  return __builtin_bit_cast(float, (u32)r[0]) + __builtin_bit_cast(float, (u32)r[1]);
}
typedef __attribute__((address_space(3))) char lchar;
DEVI u32 ldsaddr(const char* p) { return (u32)(size_t)(const lchar*)p; }
template <int N> DEVI void wait_vm() { asm volatile("s_waitcnt vmcnt(%0)" ::"n"(N) : "memory"); }
DEVI void dma_stage(char* dst, rsrc_t r, int ve, int vo, int so, int ld) {
#pragma unroll
  for (int n = 0; n < 4; ++n)
    __builtin_amdgcn_raw_ptr_buffer_load_lds(r, (lchar*)(dst + n * 1024), 16, (n & 1) ? vo : ve, so + n * 8 * ld, 0, 0);
}
DEVI int dma_voff(int lane, int ld, int odd) { return (lane >> 3) * ld + (((lane & 7) ^ ((((lane >> 4) & 1) | (odd << 1)) << 1)) << 4); }
DEVI void tr_addr(u32* ta, u32 base, int lane) {
  const int kg = lane >> 4, q = (lane >> 2) & 3, pp = lane & 3;
  const int fr = ((q >> 1) & 1) | ((kg & 1) << 1);
#pragma unroll
  for (int j = 0; j < 4; ++j) ta[j] = base + (8 * kg + q) * 128 + (((2 * j + (pp >> 1)) ^ (fr << 1)) << 4) + (pp & 1) * 8;
}
struct TrFrag { u32x2 h[8]; };
template <int OFF> DEVI void tr_issue(TrFrag& t, const u32* ta) {
  asm volatile(
      "ds_read_b64_tr_b16 %0, %8 offset:%12\n\tds_read_b64_tr_b16 %1, %8 offset:%13\n\t"
      "ds_read_b64_tr_b16 %2, %9 offset:%12\n\tds_read_b64_tr_b16 %3, %9 offset:%13\n\t"
      "ds_read_b64_tr_b16 %4, %10 offset:%12\n\tds_read_b64_tr_b16 %5, %10 offset:%13\n\t"
      "ds_read_b64_tr_b16 %6, %11 offset:%12\n\tds_read_b64_tr_b16 %7, %11 offset:%13"
      : "=&v"(t.h[0]), "=&v"(t.h[1]), "=&v"(t.h[2]), "=&v"(t.h[3]), "=&v"(t.h[4]), "=&v"(t.h[5]), "=&v"(t.h[6]), "=&v"(t.h[7])
      : "v"(ta[0]), "v"(ta[1]), "v"(ta[2]), "v"(ta[3]), "n"(OFF), "n"(OFF + 512)
      : "memory");
}
template <int N = 0> DEVI void tr_wait(TrFrag& t, u32x4* f) {
  asm volatile("s_waitcnt lgkmcnt(%8)"
               : "+v"(t.h[0]), "+v"(t.h[1]), "+v"(t.h[2]), "+v"(t.h[3]), "+v"(t.h[4]), "+v"(t.h[5]), "+v"(t.h[6]), "+v"(t.h[7])
               : "n"(N) : "memory");
#pragma unroll
  for (int j = 0; j < 4; ++j) f[j] = u32x4{t.h[2 * j].x, t.h[2 * j].y, t.h[2 * j + 1].x, t.h[2 * j + 1].y};
}
template <int OFF> DEVI void x_issue(u32x4* xf, u32 a) {
  asm volatile("ds_read_b128 %0, %4 offset:%5\n\tds_read_b128 %1, %4 offset:%6\n\tds_read_b128 %2, %4 offset:%7\n\tds_read_b128 %3, %4 offset:%8"
               : "=&v"(xf[0]), "=&v"(xf[1]), "=&v"(xf[2]), "=&v"(xf[3])
               : "v"(a), "n"(OFF), "n"(OFF + 4096), "n"(OFF + 8192), "n"(OFF + 12288) : "memory");
}
template <int OFF> DEVI void tr1(u32x2& d, u32 a) { asm volatile("ds_read_b64_tr_b16 %0, %1 offset:%2" : "=v"(d) : "v"(a), "n"(OFF) : "memory"); }
template <int OFF> DEVI void x1(u32x4& d, u32 a) { asm volatile("ds_read_b128 %0, %1 offset:%2" : "=v"(d) : "v"(a), "n"(OFF) : "memory"); }
DEVI void dma1(char* dst, rsrc_t r, int v, int so) { __builtin_amdgcn_raw_ptr_buffer_load_lds(r, (lchar*)dst, 16, v, so, 0, 0); }
DEVI u32 touch(const void* base, int bytes, int off) { return __builtin_amdgcn_raw_buffer_load_b32(mkrs(base, bytes), off, 0, 0); }
DEVI void touch_to(u32& d, const void* base, int bytes, int off) {
  const unsigned long long a = (unsigned long long)base;
  const u32x4 r = {(u32)a, (u32)(a >> 32), (u32)bytes, 0x00020000u};
  asm volatile("buffer_load_dword %0, %1, %2, 0 offen" : "+v"(d) : "v"(off), "s"(r) : "memory");
}
DEVI void keep(u32 v) { asm volatile("" ::"v"(v)); }
template <int I, int N, class Fn> DEVI void static_for(Fn&& f) {
  if constexpr (I < N) { f(std::integral_constant<int, I>{}); static_for<I + 1, N>(f); }
}
constexpr int imin(int a, int b) { return a < b ? a : b; }

constexpr int HS = 1152;
constexpr int L1_X = 0;
constexpr int PD1 = 6;
constexpr int CODE_BYTES = 34048;
constexpr int L1_R = 65536;
constexpr int RS1 = PD1 * 4096;
constexpr int L1_PH = L1_X;
constexpr int L1_WF = L1_R + 3 * RS1;
constexpr int L1_HT = L1_WF + 8192;
constexpr int L1_SIZE = L1_HT + 8 * HS;

__global__ __launch_bounds__(512, 1) void k1(const u16* __restrict__ x, const u16* __restrict__ wqkv,
                                              const u16* __restrict__ wphi, const u16* __restrict__ gw,
                                              const u16* __restrict__ w1, const u16* __restrict__ b1,
                                              const u16* __restrict__ w2, const u16* __restrict__ b2,
                                              const u16* __restrict__ wo, char* __restrict__ ws) {
  __shared__ __attribute__((aligned(16))) char lds[L1_SIZE];
  const int tid = threadIdx.x, lane = tid & 63, c = lane & 15, g = lane >> 4;
  const int w = __builtin_amdgcn_readfirstlane(tid >> 6);
  const int b = blockIdx.x, xcd = b & 7, slot = b >> 3;
  const int rb = (xcd >> 1) * 8 + (slot & 7), hg = (xcd & 1) * 4 + (slot >> 3);

  STAMP_DECL;
  STAMP(0, 0);
  u32 pf = 0u;
  if (w < 4) {
    const int l = w, hl = lane & 31;
    const int p = 8 * hg + 2 * l + (lane >> 5);
    u32x4 v0, v1, gv0, gv1;
    auto ln_load = [&]() {
      asm volatile("global_load_dwordx4 %0, %4, %6\n\tglobal_load_dwordx4 %1, %4, %6 offset:512\n\t"
                   "global_load_dwordx4 %2, %5, %7\n\tglobal_load_dwordx4 %3, %5, %7 offset:512"
                   : "=&v"(v0), "=&v"(v1), "=&v"(gv0), "=&v"(gv1)
                   : "v"((rb * 64 + p) * 1024 + hl * 16), "v"(hl * 16), "s"(x), "s"(gw) : "memory");
    };
    float xv[16], sum = 0.f, gs = 0.f, var = 0.f, mean, rs;
    u32 nn[8];
    auto ln = [&](auto kc) {
      constexpr int k = decltype(kc)::value;
      if constexpr (k == 0) asm volatile("" : "+v"(v0), "+v"(v1), "+v"(gv0), "+v"(gv1));
      if constexpr (k == 0 || k == 1) {
#pragma unroll
        for (int e = 4 * k; e < 4 * k + 4; ++e) {
          const u32 v = k ? v1[e & 3] : v0[e & 3], gg = k ? gv1[e & 3] : gv0[e & 3];
          xv[2 * e] = blo(v); xv[2 * e + 1] = bhi(v);
          sum += xv[2 * e] + xv[2 * e + 1];
          const u32 pr = pk2(xv[2 * e] * blo(gg), xv[2 * e + 1] * bhi(gg));
          gs += blo(pr) + bhi(pr);
        }
      } else if constexpr (k == 2) {
        sum = half_sum(sum) * (1.f / 512.f);
        gs = half_sum(gs);
      } else if constexpr (k == 3) {
#pragma unroll
        for (int e = 0; e < 16; ++e) { const float d = xv[e] - sum; var += d * d; }
      } else if constexpr (k == 4) {
        var = half_sum(var) * (1.f / 512.f);
        mean = rbf(sum);
        rs = rbf(sqrtf(rbf(rbf(var) + 1e-5f)));
      } else if constexpr (k == 5) {
        rs = 1.f / rs;
        gs = rbf(1.f / (1.f + __expf(-rbf(gs))));
      } else if constexpr (k == 6 || k == 7) {
#pragma unroll
        for (int e = 4 * (k - 6); e < 4 * (k - 6) + 4; ++e) {
          const u32 pr = pk2(xv[2 * e] - mean, xv[2 * e + 1] - mean);
          nn[e] = pk2(blo(pr) * rs, bhi(pr) * rs);
        }
      } else if constexpr (k == 8) {
        const u32 hp = ldsaddr(lds) + L1_HT + lane * 16 + l * 4;
#pragma unroll
        for (int t = 0; t < 4; ++t) {
          const auto q = __builtin_amdgcn_permlane32_swap(nn[t], nn[4 + t], false, false);
          const u32 lo = __builtin_amdgcn_perm((u32)q[1], (u32)q[0], 0x05040100u), hi = __builtin_amdgcn_perm((u32)q[1], (u32)q[0], 0x07060302u);
          asm volatile("ds_write_b32 %0, %1\n\tds_write_b32 %0, %2 offset:%3" ::"v"(hp + 2 * t * HS), "v"(lo), "v"(hi), "n"(HS) : "memory");
        }
      }
    };
    ln_load();
    if (w < 2) {
      u32x2 wp[8];
      const rsrc_t rp = mkrs(wphi, 32 * 256 * 2);
      const int voff = ((4 * hg + g) * 256 + 4 * (c & 3)) * 2 + w * 256;
#pragma unroll
      for (int d = 0; d < 8; ++d) wp[d] = ld8(rp, voff + d * 32, 0);
      u32x4 f[4];
      wperm(wp, f);
      const bool on = (g == (c >> 2));
#pragma unroll
      for (int jj = 0; jj < 4; ++jj) *(u32x4*)(lds + L1_WF + (jj * 2 + w) * 1024 + lane * 16) = on ? f[jj] : u32x4{0u, 0u, 0u, 0u};
    } else if (w == 2) {
#pragma unroll
      for (int i = 0; i < 3; ++i) {
        const int idx = i * 64 + lane, r = idx / 3;
        touch_to(pf, wqkv, 512 * 1536 * 2, idx < 132 ? (160 + (slot & 7) * 44 + r) * 3072 + (idx - r * 3) * 1024 + hg * 128 : 0x7fffffff);
      }
    } else {
      touch_to(pf, (const void*)(__builtin_amdgcn_s_getpc() & ~127ull), CODE_BYTES, lane < 12 ? (slot * 12 + lane) * 128 : 0x7fffffff);
      touch_to(pf, b1, F_ * 2, lane * 128);
      touch_to(pf, b2, S_ * 2, lane * 128);
    }
    wait_vm<3>();
    static_for<0, 9>(ln);
    if (hl == 0) *(float*)(ws + WS_G + (rb * 64 + p) * 4) = gs;
    u32 xa[4], ta[3];
#pragma unroll
    for (int t = 0; t < 4; ++t) xa[t] = ldsaddr(lds) + c * 256 + (((4 * t + g) ^ c) << 4);
    {
      const int q = (lane >> 2) & 3, pp = lane & 3, fr = ((q >> 1) & 1) | ((g & 1) << 1);
#pragma unroll
      for (int t = 0; t < 3; ++t) ta[t] = ldsaddr(lds) + L1_R + t * RS1 + (8 * g + q) * 128 + (((2 * w + (pp >> 1)) ^ (fr << 1)) << 4) + (pp & 1) * 8;
    }
    f32x4 acc[3][4];
#pragma unroll
    for (int j = 0; j < 12; ++j) acc[j >> 2][j & 3] = f32x4{0.f, 0.f, 0.f, 0.f};
    u32x2 tf[2][6];
    u32x4 xf[2][4];
    STAMP(0, 1);
    static_for<0, 17>([&](auto sc) {
      constexpr int s = decltype(sc)::value, u = s & 1, v = u ^ 1;
      if constexpr (s < 16) __builtin_amdgcn_s_barrier();
      if (s == 0) STAMP(0, 2);
      if (s == 8) STAMP(0, 3);
      SCHED_FENCE();
      static_for<0, 12>([&](auto mc) {
        constexpr int m = decltype(mc)::value, j = m >> 2;
        if constexpr (s > 0) acc[j][m & 3] = mfma(u32x4{tf[v][2 * j].x, tf[v][2 * j].y, tf[v][2 * j + 1].x, tf[v][2 * j + 1].y}, xf[v][m & 3], acc[j][m & 3]);
        if constexpr (s < 16 && m < 3) {
          tr1<(s % PD1) * 4096>(tf[u][2 * m], ta[m]);
          tr1<(s % PD1) * 4096 + 512>(tf[u][2 * m + 1], ta[m]);
        }
        if constexpr (s < 16 && m >= 3 && m < 5) {
          x1<(s >> 2) * 16384 + (2 * m - 6) * 4096>(xf[u][2 * m - 6], xa[s & 3]);
          x1<(s >> 2) * 16384 + (2 * m - 5) * 4096>(xf[u][2 * m - 5], xa[s & 3]);
        }
        SCHED_FENCE();
      });
      if constexpr (s < 16)
        asm volatile("s_waitcnt lgkmcnt(0)"
                     : "+v"(tf[u][0]), "+v"(tf[u][1]), "+v"(tf[u][2]), "+v"(tf[u][3]), "+v"(tf[u][4]), "+v"(tf[u][5]), "+v"(xf[u][0]),
                       "+v"(xf[u][1]), "+v"(xf[u][2]), "+v"(xf[u][3]) :: "memory");
    });
#pragma unroll
    for (int j = 0; j < 3; ++j)
#pragma unroll
      for (int t = 0; t < 4; ++t)
        *(u32x2*)(lds + L1_R + j * RS1 + (t * 2 + (g >> 1)) * HS + (16 * w + c) * 16 + (g & 1) * 8) =
            u32x2{pk2(acc[j][t][0], acc[j][t][1]), pk2(acc[j][t][2], acc[j][t][3])};
  } else {
    const int l = w - 4;
    const rsrc_t rx = mkrs(x, S_ * D_ * 2);
    auto pre = [&]() {
#pragma unroll
      for (int i = 0; i < 2; ++i) {
        const int idx = (l * 2 + i) * 64 + lane, r = idx / 7;
        touch_to(pf, w1, S_ * F_ * 2, idx < 448 ? (slot * 64 + r) * (F_ * 2) + (xcd >> 1) * 896 + (idx - r * 7) * 128 : 0x7fffffff);
      }
    };
    constexpr int NT = W2K1 ? 4 : 2;
    if (l < 3) {
      const rsrc_t rw = mkrs(wqkv, 512 * 1536 * 2);
      const int cb = (l * 512 + hg * 64) * 2;
      const int ve = dma_voff(lane, 3072, 0) + cb, vo = dma_voff(lane, 3072, 1) + cb;
      char* ring = lds + L1_R + l * RS1;
#pragma unroll
      for (int s = 0; s < PD1 - 1; ++s) dma_stage(ring + s * 4096, rw, ve, vo, s * 32 * 3072, 3072);
      STAMP(0, 1);
      static_for<0, 16>([&](auto sc) {
        constexpr int s = decltype(sc)::value, n = s + PD1 - 1;
        wait_vm<4 * imin(PD1 - 2, 15 - s) + (n > 16 ? NT : 0)>();
        __builtin_amdgcn_s_barrier();
        SCHED_FENCE();
        if constexpr (n < 16) dma_stage(ring + (n % PD1) * 4096, rw, ve, vo, n * 32 * 3072, 3072);
        if constexpr (n == 16) pre();
        SCHED_FENCE();
      });
    } else {
      auto xi = [&](int i) {
        dma1(lds + L1_X + i * 1024, rx, g * 1024 + ((c ^ (4 * (i & 3) + g)) << 4), (rb * 64 + 4 * (i & 15)) * 1024 + (i >> 4) * 256);
      };
#pragma unroll
      for (int i = 0; i < 24; ++i) xi(i);
      STAMP(0, 1);
      static_for<0, 16>([&](auto sc) {
        constexpr int s = decltype(sc)::value;
        if constexpr ((s & 3) == 0) wait_vm<(s == 0 ? 8 : s == 4 ? 24 : s == 8 ? 16 : NT)>();
        __builtin_amdgcn_s_barrier();
        SCHED_FENCE();
        if constexpr (s < 5) {
#pragma unroll
          for (int i = 0; i < 8; ++i) xi(24 + 8 * s + i);
        }
        if constexpr (s == 11) pre();
        SCHED_FENCE();
      });
    }
  }
  STAMP(0, 4);
  RAW_BARRIER();
  STAMP(0, 5);
  st16(ws, (int)WS_H + (rb * 8 + hg) * 8192 + tid * 16, *(const u32x4*)(lds + L1_HT + (lane & 7) * HS + (tid >> 3) * 16));
  {
    const int mat = 1 - (w >> 2), tau = w & 3;
    const u32x4 b0 = *(const u32x4*)(lds + L1_R + mat * RS1 + (tau * 2) * HS + lane * 16);
    const u32x4 b1 = *(const u32x4*)(lds + L1_R + mat * RS1 + (tau * 2 + 1) * HS + lane * 16);
    float p[16];
#pragma unroll
    for (int jj = 0; jj < 4; ++jj) {
      const u32x4 a0 = *(const u32x4*)(lds + L1_WF + (jj * 2) * 1024 + lane * 16);
      const u32x4 a1 = *(const u32x4*)(lds + L1_WF + (jj * 2 + 1) * 1024 + lane * 16);
      f32x4 r = mfma(a1, b1, mfma(a0, b0, f32x4{0.f, 0.f, 0.f, 0.f}));
      const u32 u0 = pk2(r[0], r[1]), u1 = pk2(r[2], r[3]);
      p[jj] = blo(u0); p[4 + jj] = bhi(u0); p[8 + jj] = blo(u1); p[12 + jj] = bhi(u1);
    }
    float mx = p[0], mn = p[0];
#pragma unroll
    for (int f = 1; f < 16; ++f) { mx = fmaxf(mx, p[f]); mn = fminf(mn, p[f]); }
    const float L2E = 1.4426950408889634f;
    float ep[16], en[16], sp = 0.f, sn = 0.f;
#pragma unroll
    for (int f = 0; f < 16; ++f) {
      ep[f] = __builtin_amdgcn_exp2f((p[f] - mx) * L2E);
      en[f] = __builtin_amdgcn_exp2f((mn - p[f]) * L2E);
      sp += ep[f]; sn += en[f];
    }
    asm volatile("" : "+v"(sp), "+v"(sn));
    if (mat == 0) RAW_BARRIER();
    asm volatile("" : "+v"(sp), "+v"(sn));
    const float rp = __builtin_amdgcn_rcpf(sp), rn = __builtin_amdgcn_rcpf(sn);
    u32x4 ph[4];
#pragma unroll
    for (int t = 0; t < 4; ++t) {
      ph[0][t] = pk2(ep[2 * t] * rp, ep[2 * t + 1] * rp);
      ph[1][t] = pk2(ep[8 + 2 * t] * rp, ep[9 + 2 * t] * rp);
      ph[2][t] = pk2(en[2 * t] * rn, en[2 * t + 1] * rn);
      ph[3][t] = pk2(en[8 + 2 * t] * rn, en[9 + 2 * t] * rn);
    }
    if (mat == 0) {
      const int dst = (int)WS_PHIQ + ((4 * hg + g) * 128 + rb * 4 + tau) * 1024 + c * 16;
#pragma unroll
      for (int pc = 0; pc < 4; ++pc) st16(ws, dst + pc * 256, ph[pc]);
    } else {
#pragma unroll
      for (int pc = 0; pc < 4; ++pc) *(u32x4*)(lds + L1_PH + (tau * 4 + pc) * HS + lane * 16) = ph[pc];
    }
  }
  STAMP(0, 6);
  if (w < 4) {
    RAW_BARRIER();
    const int hh = w;
#pragma unroll
    for (int sign = 0; sign < 2; ++sign) {
    const int q = (lane >> 2) & 3, pp = lane & 3;
    const int ro = (pp >> 1) * HS + (16 * hh + 4 * g + q) * 16 + (pp & 1) * 8;
    f32x4 kv = {0.f, 0.f, 0.f, 0.f}, z = {0.f, 0.f, 0.f, 0.f};
    const u32x4 ones = {0x3f803f80u, 0x3f803f80u, 0x3f803f80u, 0x3f803f80u};
#pragma unroll
    for (int sg = 0; sg < 2; ++sg) {
      u32x2 a0, a1, v0, v1;
      const u32 pa = ldsaddr(lds + L1_PH) + (8 * sg + 2 * sign) * HS + ro, pv = ldsaddr(lds + L1_R + 2 * RS1) + 4 * sg * HS + ro;
      asm volatile("ds_read_b64_tr_b16 %0, %4\n\tds_read_b64_tr_b16 %1, %4 offset:%6\n\tds_read_b64_tr_b16 %2, %5\n\t"
                   "ds_read_b64_tr_b16 %3, %5 offset:%7\n\ts_waitcnt lgkmcnt(0)"
                   : "=&v"(a0), "=&v"(a1), "=&v"(v0), "=&v"(v1) : "v"(pa), "v"(pv), "n"(4 * HS), "n"(2 * HS) : "memory");
      const u32x4 a = {a0.x, a0.y, a1.x, a1.y}, v = {v0.x, v0.y, v1.x, v1.y};
      kv = mfma(a, v, kv);
      z = mfma(a, ones, z);
    }
    const int hb = (4 * hg + hh) * 32 + rb;
    st16(ws, (int)WS_KVP + (hb * 512 + sign * 256 + lane * 4) * 4, __builtin_bit_cast(u32x4, kv));
    st16(ws, c == 0 ? (int)WS_ZP + (hb * 32 + sign * 16 + 4 * g) * 4 : 0x7ffffff0, __builtin_bit_cast(u32x4, z));
    }
  }
  asm volatile("s_waitcnt vmcnt(0)" : "+v"(pf) :: "memory");
  STAMP(0, 7);
  STAMP_OUT(0);
}

constexpr int NB = 5, SLOT = 32768;
struct St { u32x2 t[4][4]; u32x4 p[4][2]; };
template <int SL, int N> DEVI void st_read(St& f, const u32 (*ta)[2], const u32* pa) {
  constexpr int l = N / 6, r = N % 6, bi = SL >> 1, off = (SL & 1) * SLOT + l * 8192;
  if constexpr (r < 4) tr1<off + (r & 1) * 512>(f.t[l][r], ta[bi][r >> 1]);
  else x1<off + 4096 + (r - 4) * 1024>(f.p[l][r - 4], pa[bi]);
}
DEVI void st_wait(St& f) {
  asm volatile("s_waitcnt lgkmcnt(0)" ::: "memory");
#pragma unroll
  for (int l = 0; l < 4; ++l)
    asm volatile("" : "+v"(f.t[l][0]), "+v"(f.t[l][1]), "+v"(f.t[l][2]), "+v"(f.t[l][3]), "+v"(f.p[l][0]), "+v"(f.p[l][1]));
}
DEVI u32x4 st_w(const St& f, int l, int j) { return u32x4{f.t[l][2 * j].x, f.t[l][2 * j].y, f.t[l][2 * j + 1].x, f.t[l][2 * j + 1].y}; }
DEVI void st_addr(u32 (*ta)[2], u32* pa, u32 base, int lane, int wh, int ph) {
  const int kg = lane >> 4, q = (lane >> 2) & 3, pp = lane & 3;
  const int fr = ((q >> 1) & 1) | ((kg & 1) << 1);
#pragma unroll
  for (int bi = 0; bi < 3; ++bi) {
#pragma unroll
    for (int jj = 0; jj < 2; ++jj) ta[bi][jj] = base + bi * 65536 + (8 * kg + q) * 128 + (((4 * wh + 2 * jj + (pp >> 1)) ^ (fr << 1)) << 4) + (pp & 1) * 8;
    pa[bi] = base + bi * 65536 + ph * 2048 + lane * 16;
  }
}
template <int NS, class RD, class MM> DEVI void consume(RD&& rd, MM&& mm) {
  St f[2];
  static_for<0, NS + 1>([&](auto sc) {
    constexpr int s = decltype(sc)::value;
    if constexpr (s < NS) __builtin_amdgcn_s_barrier();
    SCHED_FENCE();
    static_for<0, 16>([&](auto mc) {
      constexpr int m = decltype(mc)::value;
      if constexpr (s > 0) mm(std::integral_constant<int, s - 1>{}, mc, f[(s - 1) & 1]);
      if constexpr (s < NS && m < 12) {
        rd(sc, std::integral_constant<int, 2 * m>{}, f[s & 1]);
        rd(sc, std::integral_constant<int, 2 * m + 1>{}, f[s & 1]);
      }
      SCHED_FENCE();
    });
    if constexpr (s < NS) st_wait(f[s & 1]);
  });
}
DEVI void dma_p(char* dst, rsrc_t r, int v, int so) {
#pragma unroll
  for (int i = 0; i < 4; ++i) dma1(dst + i * 1024, r, v, so + i * 256);
}
DEVI u32x4 pair16(u32 a0, u32 a1, u32 b0, u32 b1) {
  const auto x = __builtin_amdgcn_permlane16_swap(a0, b0, false, false);
  const auto y = __builtin_amdgcn_permlane16_swap(a1, b1, false, false);
  return u32x4{(u32)x[0], (u32)y[0], (u32)x[1], (u32)y[1]};
}

__global__ __launch_bounds__(512, 1) void k2(const u16* __restrict__ w1, const u16* __restrict__ b1, const u16* __restrict__ w2,
                                              const u16* __restrict__ wo, char* __restrict__ ws) {
  __shared__ __attribute__((aligned(16))) char lds[NB * SLOT];
  const int tid = threadIdx.x, lane = tid & 63, c = lane & 15, g = lane >> 4;
  const int w = __builtin_amdgcn_readfirstlane(tid >> 6);
  const int b = blockIdx.x, xcd = b & 7, slot = b >> 3;
  STAMP_DECL;
  STAMP(1, 0);
  if (slot < 28) {
    const int D0 = ((xcd & 1) * 4 + (slot & 3)) * 64, F0 = ((xcd >> 1) * 7 + (slot >> 2)) * 64;
    constexpr int NS = 16;
    if (w >= 4) {
      const int l = w - 4;
      const rsrc_t rw = mkrs(w1, S_ * F_ * 2), rp = mkrs(ws + WS_H, 2097152);
      const int ve = dma_voff(lane, F_ * 2, 0) + F0 * 2, vo = dma_voff(lane, F_ * 2, 1) + F0 * 2, vp = (g * 512 + D0 + c) * 16;
      const int sw0 = l * NS * 32 * F_ * 2, sp0 = l * NS * 32768;
      auto issue = [&](int s) {
        char* d = lds + (s % NB) * SLOT + l * 8192;
        dma_stage(d, rw, ve, vo, sw0 + s * 32 * F_ * 2, F_ * 2);
        dma_p(d + 4096, rp, vp, sp0 + s * 32768);
      };
#pragma unroll
      for (int s = 0; s < NB - 1; ++s) issue(s);
      STAMP(1, 1);
      u32 t0 = 0u, t1 = 0u;
      static_for<0, NS>([&](auto sc) {
        constexpr int s = decltype(sc)::value;
        wait_vm<8 * imin(NB - 2, NS - 1 - s) + (s > TS && s <= TS + NB - 1 && !W2K1 && !W2O ? 2 : 0)>();
        __builtin_amdgcn_s_barrier();
        SCHED_FENCE();
        if (s == 0) STAMP(1, 2);
        if (s == 8) STAMP(1, 3);
        if constexpr (s + NB - 1 < NS) issue(s + NB - 1);
        if constexpr (s == TS && !W2K1 && !W2O) {
          t0 = touch(w2, S_ * F_ * 2, (slot * 64 + l * 16 + (lane >> 2)) * (S_ * 2) + xcd * 512 + (lane & 3) * 128);
          t1 = touch(wo, D_ * D_ * 2, lane < 37 ? (slot * 148 + l * 37 + lane) * 128 : 0x7fffffff);
        }
        SCHED_FENCE();
      });
      STAMP(1, 4);
      keep(t0); keep(t1);
    } else {
      const int wh = w & 1, ph = w >> 1;
      const u32x2 bv0 = *(const u32x2*)(b1 + F0 + 32 * wh + 4 * g), bv1 = *(const u32x2*)(b1 + F0 + 32 * wh + 16 + 4 * g);
      u32 ta[3][2], pa[3];
      st_addr(ta, pa, ldsaddr(lds), lane, wh, ph);
      f32x4 acc[2][2];
#pragma unroll
      for (int j = 0; j < 4; ++j) acc[j >> 1][j & 1] = f32x4{0.f, 0.f, 0.f, 0.f};
      consume<NS>([&](auto sc, auto nc, St& f) { st_read<decltype(sc)::value % NB, decltype(nc)::value>(f, ta, pa); },
                  [&](auto, auto mc, St& f) {
                    constexpr int m = decltype(mc)::value;
                    acc[(m >> 1) & 1][m & 1] = mfma(st_w(f, m >> 2, (m >> 1) & 1), f.p[m >> 2][m & 1], acc[(m >> 1) & 1][m & 1]);
                  });
      STAMP(1, 4);
      const int up = (int)WS_U + ((F0 / 8 + 4 * wh + 2 * (g & 1) + (g >> 1)) * 512 + D0 + 32 * ph + c) * 16;
#pragma unroll
      for (int ii = 0; ii < 2; ++ii) {
        u32 o[4];
#pragma unroll
        for (int t = 0; t < 4; ++t) {
          const u32 bb = t < 2 ? bv0[t & 1] : bv1[t & 1];
          const u32 pr = pk2(acc[t >> 1][ii][2 * (t & 1)], acc[t >> 1][ii][2 * (t & 1) + 1]);
          const u32 ps = pk2(blo(pr) + blo(bb), bhi(pr) + bhi(bb));
          const f32x2 u = {blo(ps), bhi(ps)};
          const f32x2 z = u * (u * u * -0.1029432397f + -2.302208198f);
          const f32x2 dn = f32x2{__builtin_amdgcn_exp2f(z[0]), __builtin_amdgcn_exp2f(z[1])} + 1.f;
          o[t] = pk2(u[0] * __builtin_amdgcn_rcpf(dn[0]), u[1] * __builtin_amdgcn_rcpf(dn[1]));
        }
        st16(ws, up + ii * 256, pair16(o[0], o[1], o[2], o[3]));
      }
      STAMP(1, 5);
    }
  } else {
    const int h = xcd * 4 + (slot - 28);
    const rsrc_t rk = mkrs(ws + WS_KVP + (size_t)h * 65536, 65536), rq = mkrs(ws + WS_PHIQ + (size_t)h * 131072, 131072);
    u32x4 kp[8];
#pragma unroll
    for (int n = 0; n < 8; ++n) kp[n] = ld16(rk, (tid >> 7) * 16384 + (tid & 127) * 16, n * 2048);
    const f32x4 zp = *(const f32x4*)(ws + WS_ZP + (size_t)h * 4096 + (tid & 255) * 16);
    u32x4 ph[16];
#pragma unroll
    for (int n = 0; n < 16; ++n) ph[n] = ld16(rq, lane * 16, (w * 16 + n) * 1024);
    u32 pf[6] = {0u, 0u, 0u, 0u, 0u, 0u};
    f32x4 s = __builtin_bit_cast(f32x4, kp[0]);
#pragma unroll
    for (int n = 1; n < 8; ++n) s += __builtin_bit_cast(f32x4, kp[n]);
    *(f32x4*)(lds + tid * 16) = s;
    *(f32x4*)(lds + 8192 + tid * 16) = zp;
    RAW_BARRIER();
    if (tid < 32) {
      float t = 0.f;
#pragma unroll
      for (int n = 0; n < 32; ++n) t += *(const float*)(lds + 8192 + n * 128 + tid * 4);
      *(float*)(lds + 16384 + tid * 4) = t;
    }
    RAW_BARRIER();
    STAMP(1, 6);
    u32x4 akv, az;
    {
      const int o0 = ((g >> 1) * 256 + 2 * (g & 1) * 64 + c * 4) * 4;
      const f32x4 k0 = (*(const f32x4*)(lds + o0) + *(const f32x4*)(lds + 2048 + o0)) + (*(const f32x4*)(lds + 4096 + o0) + *(const f32x4*)(lds + 6144 + o0));
      const f32x4 k1v = (*(const f32x4*)(lds + o0 + 256) + *(const f32x4*)(lds + 2304 + o0)) + (*(const f32x4*)(lds + 4352 + o0) + *(const f32x4*)(lds + 6400 + o0));
      akv = u32x4{pk2(k0[0], k0[1]), pk2(k0[2], k0[3]), pk2(k1v[0], k1v[1]), pk2(k1v[2], k1v[3])};
      const f32x4 z0 = *(const f32x4*)(lds + 16384 + g * 32), z1 = *(const f32x4*)(lds + 16384 + g * 32 + 16);
      az = u32x4{pk2(z0[0], z0[1]), pk2(z0[2], z0[3]), pk2(z1[0], z1[1]), pk2(z1[2], z1[3])};
    }
    const int op = (int)WS_O + ((2 * h + (g >> 1)) * 2048 + w * 256 + c) * 16 + (g & 1) * 8;
#pragma unroll
    for (int n = 0; n < 16; ++n) {
      const f32x4 num = mfma(akv, ph[n], f32x4{0.f, 0.f, 0.f, 0.f});
      const f32x4 den = mfma(az, ph[n], f32x4{0.f, 0.f, 0.f, 0.f});
      const float rd = 1.f / rbf(rbf(den[0]) + 1e-6f);
      const u32 n0 = pk2(num[0], num[1]), n1 = pk2(num[2], num[3]);
      st8(ws, op + n * 256, u32x2{pk2(blo(n0) * rd, bhi(n0) * rd), pk2(blo(n1) * rd, bhi(n1) * rd)});
    }
#pragma unroll
    for (int i = 0; i < 6; ++i) keep(pf[i]);
    STAMP(1, 7);
  }
  STAMP_OUT(1);
}

__global__ __launch_bounds__(512, 1) void k3(const u16* __restrict__ w2, const u16* __restrict__ wo, const u16* __restrict__ b2,
                                              const char* __restrict__ ws, u16* __restrict__ out) {
  __shared__ __attribute__((aligned(16))) char lds[NB * SLOT];
  const int tid = threadIdx.x, lane = tid & 63, c = lane & 15, g = lane >> 4;
  const int w = __builtin_amdgcn_readfirstlane(tid >> 6);
  const int b = blockIdx.x, xcd = b & 7, slot = b >> 3;
  const int S0 = (xcd * 4 + (slot & 3)) * 64, D0 = (slot >> 2) * 64;
  constexpr int NY = 14, NA = 4, NS = NY + NA;
  STAMP_DECL;
  STAMP(2, 0);
  if (w >= 4) {
    const int l = w - 4;
    const rsrc_t rw2 = mkrs(w2, F_ * S_ * 2), ru = mkrs(ws + WS_U, 1835008);
    const rsrc_t rwo = mkrs(wo, D_ * D_ * 2), ro = mkrs(ws + WS_O, 2097152);
    const int v2e = dma_voff(lane, S_ * 2, 0) + S0 * 2, v2o = dma_voff(lane, S_ * 2, 1) + S0 * 2, vu = (g * 512 + D0 + c) * 16;
    const int voe = dma_voff(lane, D_ * 2, 0) + D0 * 2, voo = dma_voff(lane, D_ * 2, 1) + D0 * 2, vo = (g * 2048 + S0 + c) * 16;
    const int sw2 = l * NY * 32 * S_ * 2, su = l * NY * 32768, swo = l * NA * 32 * D_ * 2, so = l * NA * 131072;
    auto issue = [&](int s) {
      char* d = lds + (s % NB) * SLOT + l * 8192;
      if (s < NY) {
        dma_stage(d, rw2, v2e, v2o, sw2 + s * 32 * S_ * 2, S_ * 2);
        dma_p(d + 4096, ru, vu, su + s * 32768);
      } else {
        dma_stage(d, rwo, voe, voo, swo + (s - NY) * 32 * D_ * 2, D_ * 2);
        dma_p(d + 4096, ro, vo, so + (s - NY) * 131072);
      }
    };
#pragma unroll
    for (int s = 0; s < NB - 1; ++s) issue(s);
    STAMP(2, 1);
    static_for<0, NS>([&](auto sc) {
      constexpr int s = decltype(sc)::value;
      wait_vm<8 * imin(NB - 2, NS - 1 - s)>();
      __builtin_amdgcn_s_barrier();
      SCHED_FENCE();
      if (s == 0) STAMP(2, 2);
      if (s == 8) STAMP(2, 3);
      if constexpr (s + NB - 1 < NS) issue(s + NB - 1);
      SCHED_FENCE();
    });
    STAMP(2, 4);
  } else {
    const int dh = w & 1, sh = w >> 1;
    float gv[2], bv[2];
#pragma unroll
    for (int j = 0; j < 2; ++j) {
      gv[j] = *(const float*)(ws + WS_G + (S0 + 32 * sh + 16 * j + c) * 4);
      bv[j] = blo(b2[S0 + 32 * sh + 16 * j + c]);
    }
    u32 ty[3][2], py[3], ta[3][2], pa[3];
    st_addr(ty, py, ldsaddr(lds), lane, sh, dh);
    st_addr(ta, pa, ldsaddr(lds), lane, dh, sh);
    f32x4 ay[2][2], aa[2][2];
#pragma unroll
    for (int j = 0; j < 4; ++j) { ay[j >> 1][j & 1] = f32x4{0.f, 0.f, 0.f, 0.f}; aa[j >> 1][j & 1] = f32x4{0.f, 0.f, 0.f, 0.f}; }
    consume<NS>([&](auto sc, auto nc, St& f) {
                  constexpr int s = decltype(sc)::value;
                  if constexpr (s < NY) st_read<s % NB, decltype(nc)::value>(f, ty, py);
                  else st_read<s % NB, decltype(nc)::value>(f, ta, pa);
                },
                [&](auto sc, auto mc, St& f) {
                  constexpr int s = decltype(sc)::value, m = decltype(mc)::value, x = (m >> 1) & 1, y = m & 1;
                  if constexpr (s < NY) ay[y][x] = mfma(f.p[m >> 2][y], st_w(f, m >> 2, x), ay[y][x]);
                  else aa[x][y] = mfma(st_w(f, m >> 2, x), f.p[m >> 2][y], aa[x][y]);
                });
    STAMP(2, 4);
    const int op = ((S0 + 32 * sh + c) * 512 + D0 + 32 * dh + 16 * (g & 1) + 8 * (g >> 1)) * 2;
#pragma unroll
    for (int jj = 0; jj < 2; ++jj) {
      const float ga = gv[jj], gb = rbf(1.f - ga), t = gb * bv[jj];
      const f32x4 r0 = ga * aa[0][jj] + (gb * ay[0][jj] + t), r1 = ga * aa[1][jj] + (gb * ay[1][jj] + t);
      __builtin_amdgcn_raw_buffer_store_b128(pair16(pk2(r0[0], r0[1]), pk2(r0[2], r0[3]), pk2(r1[0], r1[1]), pk2(r1[2], r1[3])),
                                             mkrs(out, S_ * D_ * 2), op + jj * 16384, 0, OUTAUX);
    }
    STAMP(2, 5);
  }
  STAMP_OUT(2);
}

extern "C" void kernel_launch(void* const* d_in, const int* in_sizes, int n_in, void* d_out, int out_size, void* d_ws,
                              size_t ws_size, hipStream_t stream) {
  const u16* x = (const u16*)d_in[0];
  const u16* wqkv = (const u16*)d_in[1];
  const u16* wphi = (const u16*)d_in[2];
  const u16* wo = (const u16*)d_in[3];
  const u16* w1 = (const u16*)d_in[5];
  const u16* b1 = (const u16*)d_in[6];
  const u16* w2 = (const u16*)d_in[7];
  const u16* b2 = (const u16*)d_in[8];
  const u16* gw = (const u16*)d_in[9];
  char* ws = (char*)d_ws;
  k1<<<256, 512, 0, stream>>>(x, wqkv, wphi, gw, w1, b1, w2, b2, wo, ws);
  k2<<<256, 512, 0, stream>>>(w1, b1, w2, wo, ws);
  k3<<<256, 512, 0, stream>>>(w2, wo, b2, ws, (u16*)d_out);
}
```
